```python
import torch

def _softmax(x, axis=-1):
    return torch.softmax(x, dim=axis)
def _gelu(x):
    return torch.nn.functional.gelu(x, approximate="tanh")
def _silu(x):
    return torch.nn.functional.silu(x)
def _sigmoid(x):
    return torch.sigmoid(x)
def _layernorm(x, axis=-1, eps=1e-5):
    m = x.mean(dim=axis, keepdim=True)
    v = x.var(dim=axis, keepdim=True, unbiased=False)
    return (x - m) / torch.sqrt(v + eps)
def _scatter_add(tgt, idx, src, axis=0):
    return tgt.clone().index_add_(axis, idx, src)
def _segment_sum(src, seg, n):
    out = torch.zeros((n,) + src.shape[1:], dtype=src.dtype)
    return out.index_add_(0, seg, src)
def _pad(x, pw):
    flat = tuple(v for pair in reversed(pw) for v in pair)
    return torch.nn.functional.pad(x, flat)

kernel_name = 'interleave_layers__sliding_local_dilated__moe_arch_mod__prompt'

def setup_inputs():

    _w = (lambda a: (lambda t: t.to(torch.bfloat16) if t.is_floating_point() else t)(torch.as_tensor(a)))
    def _A_setup():
        B, S, D = 1, 2048, 512
        return {
            "x": _w((lambda *s: torch.randn(*s, dtype=torch.bfloat16) * 0.05)(B, S, D)),
            "wqkv": _w((lambda *s: torch.randn(*s, dtype=torch.bfloat16) * 0.05)(D, 3 * D)),
            "wo": _w((lambda *s: torch.randn(*s, dtype=torch.bfloat16) * 0.05)(D, D)),
        }
    _A_in = _A_setup()

    def _B_setup():
        B, S, D, F, C = 1, 2048, 512, 1376, 2
        return {
            "x": _w((lambda *s: torch.randn(*s, dtype=torch.bfloat16) * 0.05)(B, S, D)),
            "w_r": _w((lambda *s: torch.randn(*s, dtype=torch.bfloat16) * 0.05)(D)),
            "w_gate": _w((lambda *s: torch.randn(*s, dtype=torch.bfloat16) * 0.05)(D, F)),
            "w_up": _w((lambda *s: torch.randn(*s, dtype=torch.bfloat16) * 0.05)(D, F)),
            "w_down": _w((lambda *s: torch.randn(*s, dtype=torch.bfloat16) * 0.05)(F, D)),
        }
    _B_in = _B_setup()

    return {
        "A_x": _A_in["x"],
        "A_wqkv": _A_in["wqkv"],
        "A_wo": _A_in["wo"],
        "B_x": _B_in["x"],
        "B_w_r": _B_in["w_r"],
        "B_w_gate": _B_in["w_gate"],
        "B_w_up": _B_in["w_up"],
        "B_w_down": _B_in["w_down"],
    }

def reference(A_x, A_wqkv, A_wo, B_x, B_w_r, B_w_gate, B_w_up, B_w_down):
    def _A_ref(x, wqkv, wo):
        B, S, D = x.shape
        H = 32; Dh = D // H
        W, dil = 8, 2
        qkv = (lambda x, *d: x.permute(*d))((x @ wqkv).reshape(B, S, 3, H, Dh), 2, 0, 3, 1, 4)
        q, k, v = qkv[0], qkv[1], qkv[2]
        scale = Dh ** -0.5
        pos = torch.arange(S)
        off = torch.arange(W) * dil
        idx = pos[:, None] - off[None, :]
        mask = idx >= 0
        idx_c = torch.where(mask, idx, 0).reshape(-1)
        kg = k[:, :, idx_c, :].reshape(B, H, S, W, Dh)
        vg = v[:, :, idx_c, :].reshape(B, H, S, W, Dh)
        logits = torch.einsum("bhsd,bhswd->bhsw", q, kg) * scale
        logits = torch.where(mask[None, None, :, :], logits, -1e9)
        attn = _softmax(logits, axis=-1)
        o = torch.einsum("bhsw,bhswd->bhsd", attn, vg)
        return (lambda x, *d: x.permute(*d))(o, 0, 2, 1, 3).reshape(B, S, D) @ wo

    def _B_ref(x, w_r, w_gate, w_up, w_down):
        B, S, D = x.shape
        C = 2
        r = x @ w_r
        idx = torch.argsort(-r, axis=-1)[:, :C]
        idx_e = (lambda x, n, axis: torch.repeat_interleave(x, n, dim=axis))(idx.reshape(B, C, 1), D, axis=2)
        x_sel = (lambda x, i, axis: torch.take_along_dim(x, i, dim=axis))(x, idx_e, axis=1)
        g = _silu(x_sel @ w_gate)
        u = x_sel @ w_up
        y = (g * u) @ w_down
        r_sel = _sigmoid((lambda x, i, axis: torch.take_along_dim(x, i, dim=axis))(r, idx, axis=1))
        y = y * r_sel.reshape(B, C, 1)
        b_off = (torch.arange(B) * S).reshape(B, 1)
        flat_idx = (idx + b_off).reshape(B * C)
        zero = (x * 0.0).reshape(B * S, D)
        upd = _scatter_add(zero, flat_idx, y.reshape(B * C, D), axis=0)
        return x + upd.reshape(B, S, D)

    x = A_x
    x = x + _A_ref(_layernorm(x, axis=-1), A_wqkv, A_wo)
    x = x + _B_ref(_layernorm(x, axis=-1), B_w_r, B_w_gate, B_w_up, B_w_down)
    x = x + _A_ref(_layernorm(x, axis=-1), A_wqkv, A_wo)
    x = x + _B_ref(_layernorm(x, axis=-1), B_w_r, B_w_gate, B_w_up, B_w_down)
    return x
```

```cpp
#include <hip/hip_runtime.h>
#include <hip/hip_bf16.h>
typedef unsigned short u16;
static __device__ __forceinline__ float bf2f(u16 b){ return __builtin_bit_cast(float,(unsigned)b<<16); }
static __device__ __forceinline__ u16 f2bf(float f){ unsigned u=__builtin_bit_cast(unsigned,f); u+=0x7FFFu+((u>>16)&1u); return (u16)(u>>16);}
constexpr int S=2048,D=512,H=32,F=1376;
__global__ void k_cvt(const u16* x, float* y, int n){int i=blockIdx.x*256+threadIdx.x; if(i<n) y[i]=bf2f(x[i]);}
__global__ void k_ln(const float* x, float* h, const u16* wr, float* r){
  int t=blockIdx.x, l=threadIdx.x; float v[8]; float s=0; for(int j=0;j<8;j++){v[j]=x[t*D+l+64*j]; s+=v[j];}
  for(int o=32;o;o>>=1) s+=__shfl_xor(s,o); float m=s/D; float q=0; for(int j=0;j<8;j++){q+=(v[j]-m)*(v[j]-m);}
  for(int o=32;o;o>>=1) q+=__shfl_xor(q,o); float rs=1.f/sqrtf(q/D+1e-5f); float d=0;
  for(int j=0;j<8;j++){float hv=(v[j]-m)*rs; h[t*D+l+64*j]=hv; d+=hv*bf2f(wr[l+64*j]);}
  for(int o=32;o;o>>=1) d+=__shfl_xor(d,o); if(l==0) r[t]=d; }
__global__ void k_gemm(const float* A,const u16* W,float* C,const float* res,int M,int N,int K){
  __shared__ float As[16][17], Ws[16][17]; int tx=threadIdx.x&15, ty=threadIdx.x>>4; int row=blockIdx.y*16+ty, col=blockIdx.x*16+tx; float acc=0;
  for(int k0=0;k0<K;k0+=16){ As[ty][tx]=A[row*K+k0+tx]; Ws[ty][tx]=bf2f(W[(k0+ty)*N+col]); __syncthreads(); for(int k=0;k<16;k++) acc+=As[ty][k]*Ws[k][tx]; __syncthreads(); }
  C[row*N+col]=acc+(res?res[row*N+col]:0.f); }
__global__ void k_attn(const float* qkv,float* o){ int i=blockIdx.x*256+threadIdx.x; int t=i/H,h=i%H; float q[16]; for(int d=0;d<16;d++) q[d]=qkv[t*1536+h*16+d];
  float lg[8]; float mx=-1e30f; for(int w=0;w<8;w++){int s=t-2*w; float a=-1e9f; if(s>=0){a=0; for(int d=0;d<16;d++) a+=q[d]*qkv[s*1536+512+h*16+d]; a*=0.25f;} lg[w]=a; mx=fmaxf(mx,a);}
  float sum=0; for(int w=0;w<8;w++){lg[w]=__expf(lg[w]-mx); sum+=lg[w];}
  for(int d=0;d<16;d++){float a=0; for(int w=0;w<8;w++){int s=t-2*w; if(s<0)s=0; a+=lg[w]*qkv[s*1536+1024+h*16+d];} o[t*D+h*16+d]=a/sum;} }
__global__ void k_top2(const float* r,int* idx){ if(threadIdx.x==0){ int b0=-1,b1=-1; float v0=-1e30f,v1=-1e30f; for(int t=0;t<S;t++){float v=r[t]; if(v>v0){v1=v0;b1=b0;v0=v;b0=t;} else if(v>v1){v1=v;b1=t;}} idx[0]=b0; idx[1]=b1;} }
__global__ void k_gu(const float* h,const int* idx,const u16* wg,const u16* wu,float* hid){ int f=blockIdx.x*64+threadIdx.x; int c=blockIdx.y; if(f>=F) return; const float* hr=h+idx[c]*D; float g=0,u=0; for(int k=0;k<D;k++){g+=hr[k]*bf2f(wg[k*F+f]); u+=hr[k]*bf2f(wu[k*F+f]);} hid[c*F+f]=g/(1.f+__expf(-g))*u; }
__global__ void k_down(const float* hid,const int* idx,const float* r,const u16* wd,float* y){ int n=blockIdx.x*64+threadIdx.x; int c=blockIdx.y; float a=0; for(int f=0;f<F;f++) a+=hid[c*F+f]*bf2f(wd[f*D+n]); float rr=r[idx[c]]; y[c*D+n]=a/(1.f+__expf(-rr)); }
__global__ void k_res(const float* x,const float* h,const int* idx,const float* y,float* xo,u16* out){ int i=blockIdx.x*256+threadIdx.x; int t=i/D,n=i%D; float v=x[i]+h[i]; if(t==idx[0]) v+=y[n]; if(t==idx[1]) v+=y[D+n]; if(xo) xo[i]=v; if(out) out[i]=f2bf(v);}
extern "C" void kernel_launch(void* const* d_in, const int* in_sizes, int n_in, void* d_out, int out_size, void* d_ws, size_t ws_size, hipStream_t stream){
  const u16* x=(const u16*)d_in[0]; const u16* wqkv=(const u16*)d_in[1]; const u16* wo=(const u16*)d_in[2]; const u16* wr=(const u16*)d_in[4]; const u16* wg=(const u16*)d_in[5]; const u16* wu=(const u16*)d_in[6]; const u16* wd=(const u16*)d_in[7];
  float* ws=(float*)d_ws; float* xa=ws; float* xb=xa+S*D; float* h=xb+S*D; float* qkv=h+S*D; float* o=qkv+S*1536; float* r=o+S*D; float* hid=r+S; float* y=hid+2*F; int* idx=(int*)(y+2*D);
  k_cvt<<<S*D/256,256,0,stream>>>(x,xa,S*D);
  for(int l=0;l<2;l++){
    k_ln<<<S,64,0,stream>>>(xa,h,wr,r);
    k_gemm<<<dim3(1536/16,S/16),256,0,stream>>>(h,wqkv,qkv,nullptr,S,1536,D);
    k_attn<<<S*H/256,256,0,stream>>>(qkv,o);
    k_gemm<<<dim3(D/16,S/16),256,0,stream>>>(o,wo,xb,xa,S,D,D);
    k_ln<<<S,64,0,stream>>>(xb,h,wr,r);
    k_top2<<<1,64,0,stream>>>(r,idx);
    k_gu<<<dim3((F+63)/64,2),64,0,stream>>>(h,idx,wg,wu,hid);
    k_down<<<dim3(D/64,2),64,0,stream>>>(hid,idx,r,wd,y);
    k_res<<<S*D/256,256,0,stream>>>(xb,h,idx,y,l==0?xa:nullptr,l==1?(u16*)d_out:nullptr);
  }
}
```

```cpp
#include <hip/hip_runtime.h>

namespace {
typedef unsigned short u16;
typedef unsigned long long u64;
typedef unsigned v4u __attribute__((ext_vector_type(4)));
typedef unsigned v2u __attribute__((ext_vector_type(2)));
typedef float f32x4 __attribute__((ext_vector_type(4)));
typedef float f32x2 __attribute__((ext_vector_type(2)));
typedef __bf16 bf16x8 __attribute__((ext_vector_type(8)));
typedef __bf16 bf16x2 __attribute__((ext_vector_type(2)));
typedef short s16x4 __attribute__((ext_vector_type(4)));
typedef __amdgpu_buffer_rsrc_t rsrc_t;
#define LAS __attribute__((address_space(3)))
#define DEV __device__ __forceinline__

#define MAPPING 1

constexpr int W_EPOCH = 0, W_FLAGS = 4096, W_KEY = 16384, W_ST = 32768, W_PST = 65536, W_Y = 393216;
constexpr int KEY_L = 8192, ST_L = 16384, PST_L = 131072, Y_L = 98304;
constexpr int W_O = 1 << 20, W_X = 5 << 20, BIG_L = 2 << 20, W_END = 9 << 20;
constexpr int W_DBG = 16 << 20;
constexpr unsigned OOB = 0x7ff00000u;
constexpr unsigned POISON = 0xFFFFFFFFu;
constexpr int NFF = 43;
constexpr int SPIN = 1 << 15;
constexpr float EPS = 1e-5f;
constexpr int XIMG = 0, RING = 81920, MISC = 155648, LDS_BYTES = 163840;
constexpr int M_STM = MISC, M_STA = MISC + 320, M_RED = MISC + 1024, M_PS = MISC + 3072, M_TOP = MISC + 3328, M_HF = MISC + 3584,
              M_FIX = MISC + 4096, M_FST = MISC + 5120, M_YS = MISC + 5376, M_DBG = MISC + 6144, M_CS = MISC + 7168;

__shared__ __attribute__((aligned(16))) char smem[LDS_BYTES];
template <class T> DEV LAS T* lp(unsigned off) { return (LAS T*)((LAS char*)smem + off); }

#define MEMF() asm volatile("" ::: "memory")
#define WAITV(n) asm volatile("s_waitcnt vmcnt(" #n ")" ::: "memory")
#define WAITL() asm volatile("s_waitcnt lgkmcnt(0)" ::: "memory")
#define DRAIN() WAITV(0)
#define BAR() do { WAITL(); __builtin_amdgcn_s_barrier(); MEMF(); } while (0)
#define SB() __builtin_amdgcn_sched_barrier(0)
template <int N> DEV void waitv() { asm volatile("s_waitcnt vmcnt(%0)" ::"n"(N) : "memory"); }

#define STAMP(i)

DEV v4u mk_srd(const void* p, unsigned bytes) { const u64 a = (u64)p; return v4u{(unsigned)a, (unsigned)(a >> 32) & 0xffffu, bytes, 0x00020000u}; }
DEV rsrc_t mk_rs(const void* p, unsigned bytes) { return __builtin_amdgcn_make_buffer_rsrc((void*)p, 0, bytes, 0x00020000); }
template <int LI, int SI> DEV void dma(v4u srd, unsigned vo, unsigned lb, unsigned sb) {
  unsigned t;
  asm volatile("s_add_i32 m0, %3, %5\n\ts_add_i32 %0, %4, %6\n\tbuffer_load_dwordx4 %1, %2, %0 offen lds"
               : "=&s"(t) : "v"(vo), "s"(srd), "s"(lb), "s"(sb), "n"(LI), "n"(SI) : "memory", "scc");
}
DEV void dma4_sc1(v4u srd, unsigned vo, unsigned lb, unsigned sb) {
  asm volatile("s_mov_b32 m0, %2\n\ts_nop 0\n\tbuffer_load_dwordx4 %0, %1, %3 offen sc1 lds\n\tbuffer_load_dwordx4 %0, %1, %3 offen offset:1024 sc1 lds\n\t"
               "buffer_load_dwordx4 %0, %1, %3 offen offset:2048 sc1 lds\n\tbuffer_load_dwordx4 %0, %1, %3 offen offset:3072 sc1 lds"
               ::"v"(vo), "s"(srd), "s"(lb), "s"(sb) : "memory");
}
DEV v4u ldc4(rsrc_t r, unsigned vo, unsigned so = 0) { return __builtin_amdgcn_raw_buffer_load_b128(r, vo, so, 16); }
DEV v2u ldc2(rsrc_t r, unsigned vo, unsigned so = 0) { return __builtin_amdgcn_raw_buffer_load_b64(r, vo, so, 16); }
DEV unsigned ldc1(rsrc_t r, unsigned vo, unsigned so = 0) { return __builtin_amdgcn_raw_buffer_load_b32(r, vo, so, 16); }
DEV void stc4(rsrc_t r, unsigned vo, v4u v, unsigned so = 0) { __builtin_amdgcn_raw_buffer_store_b128(v, r, vo, so, 16); }
DEV void stc2(rsrc_t r, unsigned vo, v2u v, unsigned so = 0) { __builtin_amdgcn_raw_buffer_store_b64(v, r, vo, so, 16); }
DEV v4u ldp4(rsrc_t r, unsigned vo, unsigned so = 0) { return __builtin_amdgcn_raw_buffer_load_b128(r, vo, so, 0); }

#define MF_(op, pre, d, a, b, ca, cb) asm volatile(pre op " %0, %1, %2, %0" : "+v"(d) : ca(a), cb(b))
#define MFZ_(op, pre, d, a, b, ca, cb) asm volatile(pre op " %0, %1, %2, 0" : "=&v"(d) : ca(a), cb(b))
#define MFC_(op, pre, d, a, b, c) asm volatile(pre op " %0, %1, %2, %3" : "=&v"(d) : "v"(a), "v"(b), "v"(c))
#define M32 "v_mfma_f32_16x16x32_bf16"
#define M16 "v_mfma_f32_16x16x16_bf16"
#define NOP2 "s_nop 1\n\t"
constexpr int AW(int s, int p) { return (s * 3 + p) * 4; }
constexpr int AO(int j, int ct) { return 192 + (j * 2 + ct) * 4; }
template <bool Z, int A> DEV void mf_av(f32x4& d, const v4u& b) {
  if constexpr (Z) asm volatile(M32 " %0, a[%c2:%c3], %1, 0" : "=&v"(d) : "v"(b), "n"(A), "n"(A + 3));
  else asm volatile(M32 " %0, a[%c2:%c3], %1, %0" : "+v"(d) : "v"(b), "n"(A), "n"(A + 3));
}
template <bool Z, int A> DEV void mf_va(f32x4& d, const v4u& b) {
  if constexpr (Z) asm volatile(M32 " %0, %1, a[%c2:%c3], 0" : "=&v"(d) : "v"(b), "n"(A), "n"(A + 3));
  else asm volatile(M32 " %0, %1, a[%c2:%c3], %0" : "+v"(d) : "v"(b), "n"(A), "n"(A + 3));
}
template <int A, int O0, int O1> DEV void trd_a(unsigned base) {
  static_assert(O0 < 65536 && O1 < 65536, "DS offset");
  asm volatile("ds_read_b64_tr_b16 a[%c1:%c2], %0 offset:%c5\n\tds_read_b64_tr_b16 a[%c3:%c4], %0 offset:%c6"
               ::"v"(base), "n"(A), "n"(A + 1), "n"(A + 2), "n"(A + 3), "n"(O0), "n"(O1) : "memory");
}
#define A8(n) "a" #n "0", "a" #n "1", "a" #n "2", "a" #n "3", "a" #n "4", "a" #n "5", "a" #n "6", "a" #n "7", "a" #n "8", "a" #n "9"
#define CLAIM_AGPRS() asm volatile("" ::: A8(), A8(1), A8(2), A8(3), A8(4), A8(5), A8(6), A8(7), A8(8), A8(9), A8(10), A8(11), A8(12), A8(13), A8(14), A8(15), A8(16), \
    A8(17), A8(18), A8(19), A8(20), A8(21), A8(22), A8(23), A8(24), "a250", "a251", "a252", "a253", "a254", "a255")
template <bool Z> DEV void mf_vv(f32x4& d, const v4u& a, const v4u& b) { if constexpr (Z) MFZ_(M32, "", d, a, b, "v", "v"); else MF_(M32, "", d, a, b, "v", "v"); }
template <bool Z> DEV void mf_vvn(f32x4& d, const v4u& a, const v4u& b) { if constexpr (Z) MFZ_(M32, NOP2, d, a, b, "v", "v"); else MF_(M32, NOP2, d, a, b, "v", "v"); }
#define TIE1(n, a) asm volatile("s_nop " #n : "+v"(a))
#define TIE2(n, a, b) asm volatile("s_nop " #n : "+v"(a), "+v"(b))
#define TIE3(n, a, b, c) asm volatile("s_nop " #n : "+v"(a), "+v"(b), "+v"(c))
#define TIE4(n, a, b, c, d) asm volatile("s_nop " #n : "+v"(a), "+v"(b), "+v"(c), "+v"(d))
#define TIE0_2(a, b) asm volatile("" : "+v"(a), "+v"(b))
#define TIE0_4(a, b, c, d) asm volatile("" : "+v"(a), "+v"(b), "+v"(c), "+v"(d))
DEV v2u trd(unsigned off) { return __builtin_bit_cast(v2u, __builtin_amdgcn_ds_read_tr16_b64_v4i16(lp<s16x4>(off))); }
DEV v4u trd2(unsigned o0, unsigned o1) { const v2u a = trd(o0), b = trd(o1); return v4u{a.x, a.y, b.x, b.y}; }
DEV unsigned pk(float a, float b) { return __builtin_bit_cast(unsigned, __builtin_convertvector((f32x2{a, b}), bf16x2)); }
DEV float blo(unsigned v) { return __builtin_bit_cast(float, v << 16); }
DEV float bhi(unsigned v) { return __builtin_bit_cast(float, v & 0xffff0000u); }
DEV float dot2(unsigned a, unsigned b, float c) { return __builtin_amdgcn_fdot2_f32_bf16(__builtin_bit_cast(bf16x2, a), __builtin_bit_cast(bf16x2, b), c, false); }
DEV float f_(unsigned v) { return __builtin_bit_cast(float, v); }
DEV unsigned u_(float v) { return __builtin_bit_cast(unsigned, v); }
template <int C> DEV unsigned dppu(unsigned v) { return (unsigned)__builtin_amdgcn_update_dpp(0, (int)v, C, 0xF, 0xF, true); }
template <int C> DEV float dppf(float v) { return f_(dppu<C>(u_(v))); }
DEV float rowsum(float v) { v += dppf<0x128>(v); v += dppf<0x124>(v); v += dppf<0x122>(v); v += dppf<0x121>(v); return v; }
DEV unsigned rowmaxu(unsigned v) { v = max(v, dppu<0x128>(v)); v = max(v, dppu<0x124>(v)); v = max(v, dppu<0x122>(v)); v = max(v, dppu<0x121>(v)); return v; }
DEV float x16sum(float v) { auto r = __builtin_amdgcn_permlane16_swap(u_(v), u_(v), false, false); return f_(r[0]) + f_(r[1]); }
DEV float x32sum(float v) { auto r = __builtin_amdgcn_permlane32_swap(u_(v), u_(v), false, false); return f_(r[0]) + f_(r[1]); }
DEV float x16max(float v) { auto r = __builtin_amdgcn_permlane16_swap(u_(v), u_(v), false, false); return fmaxf(f_(r[0]), f_(r[1])); }
DEV float x32max(float v) { auto r = __builtin_amdgcn_permlane32_swap(u_(v), u_(v), false, false); return fmaxf(f_(r[0]), f_(r[1])); }
DEV unsigned x16maxu(unsigned v) { auto r = __builtin_amdgcn_permlane16_swap(v, v, false, false); return max((unsigned)r[0], (unsigned)r[1]); }
DEV unsigned x32maxu(unsigned v) { auto r = __builtin_amdgcn_permlane32_swap(v, v, false, false); return max((unsigned)r[0], (unsigned)r[1]); }
DEV unsigned rfl(unsigned v) { return (unsigned)__builtin_amdgcn_readfirstlane((int)v); }
DEV float rsq(float v) { return __builtin_amdgcn_rsqf(v); }
DEV float rcp(float v) { return __builtin_amdgcn_rcpf(v); }
DEV float ex2(float v) { return __builtin_amdgcn_exp2f(v); }
DEV bool isnan_(float v) { return v != v; }
constexpr unsigned ONE2 = 0x3F803F80u;
constexpr float LOG2E = 1.4426950408889634f;

template <int S> struct IC { static constexpr int v = S; };
template <int A, int B, class F> DEV void sfor(F&& f) { if constexpr (A < B) { f(IC<A>{}); sfor<A + 1, B>(f); } }

struct Ctx {
  int lane, w, t, g, c, hg, c2, u, q, ff;
  rsrc_t rS;
  DEV void fresh() { asm volatile("" : "+v"(lane)); t = lane & 15; g = lane >> 4; }
};

struct Acc { f32x4 Q[5], K[5], V[5]; };

DEV void attention(Ctx& x, const Acc& a, int L) {
  x.fresh();
  const int t = x.t, g = x.g;
  const f32x4 csq = *lp<f32x4>(M_CS + x.w * 192 + g * 16), csk = *lp<f32x4>(M_CS + x.w * 192 + 64 + g * 16);
  const float csv = *lp<float>(M_CS + x.w * 192 + 128 + t * 4);
  v2u Qp[5], Kp[5], Vp[5];
  float cq[5];
#pragma unroll
  for (int i = 0; i < 5; ++i) {
    const float m = *lp<float>(M_STM + (16 * i + t) * 4), s = *lp<float>(M_STA + (16 * i + t) * 4);
    const f32x4 m4 = *lp<f32x4>(M_STM + (16 * i + 4 * g) * 4), s4 = *lp<f32x4>(M_STA + (16 * i + 4 * g) * 4);
    const f32x4 k = (a.K[i] - m * csk) * s;
    Kp[i] = v2u{pk(k[0], k[1]), pk(k[2], k[3])};
    const f32x4 v = (a.V[i] - m4 * csv) * s4;
    Vp[i] = v2u{pk(v[0], v[1]), pk(v[2], v[3])};
    if (i > 0) { const f32x4 qq = a.Q[i] - m * csq; Qp[i] = v2u{pk(qq[0], qq[1]), pk(qq[2], qq[3])}; cq[i] = s * (0.25f * LOG2E); }
  }
  f32x4 bp, bc;
#pragma unroll
  for (int r = 0; r < 4; ++r) {
    const int kk = 4 * g + r; const bool par = ((t ^ r) & 1) == 0;
    bc[r] = (par && kk <= t) ? 0.f : -1e30f;
    bp[r] = (par && kk >= t + 2) ? 0.f : -1e30f;
  }
  const v4u ones = {ONE2, ONE2, ONE2, ONE2};
  f32x4 b0 = bp;
  if (x.c == 0) b0 = f32x4{-1e30f, -1e30f, -1e30f, -1e30f};
  f32x4 sp[5], sc[5], o[5], l[5];
#pragma unroll
  for (int i = 1; i < 5; ++i) {
    if (i == 1) MFC_(M16, NOP2, sp[i], Kp[i - 1], Qp[i], b0); else MFC_(M16, NOP2, sp[i], Kp[i - 1], Qp[i], bp);
    MFC_(M16, NOP2, sc[i], Kp[i], Qp[i], bc);
  }
  v2u Op[5];
#pragma unroll
  for (int i = 1; i < 5; ++i) {
    TIE0_2(sp[i], sc[i]);
    float mx = fmaxf(fmaxf(fmaxf(sp[i][0], sp[i][1]), fmaxf(sp[i][2], sp[i][3])), fmaxf(fmaxf(sc[i][0], sc[i][1]), fmaxf(sc[i][2], sc[i][3])));
    mx = x32max(x16max(mx));
    const float cm = -cq[i] * mx;
    const f32x4 ep = sp[i] * cq[i] + cm, ec = sc[i] * cq[i] + cm;
    const v4u P = {pk(ex2(ep[0]), ex2(ep[1])), pk(ex2(ep[2]), ex2(ep[3])), pk(ex2(ec[0]), ex2(ec[1])), pk(ex2(ec[2]), ex2(ec[3]))};
    const v4u VV = {Vp[i - 1].x, Vp[i - 1].y, Vp[i].x, Vp[i].y};
    mf_vvn<true>(l[i], ones, P);
    mf_vvn<true>(o[i], VV, P);
    if (i > 1) {
      TIE0_2(o[i - 1], l[i - 1]);
      const f32x4 on = o[i - 1] * rcp(l[i - 1][0]);
      Op[i - 1] = v2u{pk(on[0], on[1]), pk(on[2], on[3])};
    }
  }
  TIE2(7, o[4], l[4]);
  { const f32x4 on = o[4] * rcp(l[4][0]); Op[4] = v2u{pk(on[0], on[1]), pk(on[2], on[3])}; }
  const unsigned ob = W_O + L * BIG_L + (x.c * 64 + 4 * x.hg + x.w) * 1024;
  stc4(x.rS, x.lane * 16, v4u{Op[1].x, Op[1].y, Op[2].x, Op[2].y}, ob);
  stc4(x.rS, x.lane * 16, v4u{Op[3].x, Op[3].y, Op[4].x, Op[4].y}, ob + 32 * 1024);
}

DEV void top2(const Ctx& x, int L, unsigned& k1, unsigned& k2, bool nap) {
  if (x.w == 0) {
    v2u k;
    for (int sp = 0; sp < SPIN; ++sp) {
      MEMF();
      k = ldc2(x.rS, x.lane * 128, W_KEY + L * KEY_L);
      if (__all(k.x != POISON && k.y != POISON)) break;
      if (nap) __builtin_amdgcn_s_sleep(4);
    }
    const unsigned m1 = x32maxu(x16maxu(rowmaxu(k.x)));
    const unsigned m2 = x32maxu(x16maxu(rowmaxu(k.x == m1 ? k.y : k.x)));
    *lp<v2u>(M_TOP) = v2u{m1, m2};
  }
  BAR();
  const v2u k = *lp<v2u>(M_TOP);
  k1 = rfl(k.x); k2 = rfl(k.y);
}
DEV int key_tok(unsigned k) { return 2047 - (int)(k & 0x7FFu); }
DEV float key_sig(unsigned k) {
  unsigned u = (k & ~0x7FFu) | 0x400u;
  u = (u & 0x80000000u) ? (u ^ 0x80000000u) : ~u;
  return rcp(1.f + ex2(-LOG2E * f_(u)));
}

struct Ffn { v4u GU[4][4]; v4u WD[8]; float cs[4]; };
DEV void ffn_load_down(const Ctx& x, rsrc_t rD, v4u (&raw)[8]) {
#pragma unroll
  for (int e = 0; e < 8; ++e) raw[e] = ldp4(rD, (8 * x.g + e) * 1024 + x.t * 16, x.ff * 32768 + x.w * 256);
}
DEV void ffn_prep(const Ctx& x, const v4u (&raw)[8], Ffn& F) {
  const int qq = x.t >> 2, p = x.lane & 3;
  const unsigned b0 = RING + (128 * x.w + 8 * x.g + qq) * 64 + (x.g & 1) * 32 + 8 * p, b1 = b0 ^ 32;
  const v4u ones = {ONE2, ONE2, ONE2, ONE2};
#pragma unroll
  for (int c = 0; c < 8; ++c)
#pragma unroll
    for (int d = 0; d < 4; ++d) F.WD[c][d] = __builtin_amdgcn_perm(raw[2 * d + 1][c >> 1], raw[2 * d][c >> 1], (c & 1) ? 0x07060302u : 0x05040100u);
  TIE0_4(F.WD[0], F.WD[1], F.WD[2], F.WD[3]); TIE0_4(F.WD[4], F.WD[5], F.WD[6], F.WD[7]);
  SB();
  f32x4 cs[4];
#pragma unroll
  for (int s = 0; s < 4; ++s)
#pragma unroll
    for (int m = 0; m < 4; ++m) {
      const unsigned o = ((m & 1) ? b1 : b0) + s * 2048 + (m >> 1) * 32768;
      F.GU[s][m] = trd2(o, o + 256);
    }
#pragma unroll
  for (int s = 0; s < 4; ++s)
#pragma unroll
    for (int m = 0; m < 4; ++m) { if (s == 0) mf_vvn<true>(cs[m], ones, F.GU[s][m]); else mf_vv<false>(cs[m], ones, F.GU[s][m]); }
  TIE4(7, cs[0], cs[1], cs[2], cs[3]);
#pragma unroll
  for (int m = 0; m < 4; ++m) F.cs[m] = cs[m][0];
}
DEV void ffn_run(const Ctx& x, const Ffn& F, int L, unsigned k1, unsigned k2) {
  const int ta = key_tok(k1), tb = key_tok(k2);
  const int tk = ((x.t >> 2) & 1) ? tb : ta;
  const unsigned xo = ((tk >> 4) * 16 + 4 * x.w) * 1024 + (16 * x.g + (tk & 15)) * 16;
  v4u xf[4]; v2u sa, sb;
  for (int sp = 0; sp < SPIN; ++sp) {
    MEMF();
#pragma unroll
    for (int s = 0; s < 4; ++s) xf[s] = ldc4(x.rS, xo + s * 1024, W_X + L * BIG_L);
    sa = ldc2(x.rS, ta * 8, W_ST + L * ST_L); sb = ldc2(x.rS, tb * 8, W_ST + L * ST_L);
    const unsigned m = max(max(max(xf[0].x, xf[1].x), max(xf[2].x, xf[3].x)), max(sa.x, sb.x));
    if (__all(m != POISON)) break;
  }
  f32x4 d[4];
#pragma unroll
  for (int s = 0; s < 4; ++s)
#pragma unroll
    for (int m = 0; m < 4; ++m) { if (s == 0) mf_vvn<true>(d[m], xf[s], F.GU[s][m]); else mf_vv<false>(d[m], xf[s], F.GU[s][m]); }
  TIE4(7, d[0], d[1], d[2], d[3]);
  const float mt = (x.g & 1) ? f_(sb.x) : f_(sa.x);
  const bool hi = x.g >= 2;
  const float vg = (hi ? d[1][0] : d[0][0]) - mt * (hi ? F.cs[1] : F.cs[0]);
  const float vu = (hi ? d[3][0] : d[2][0]) - mt * (hi ? F.cs[3] : F.cs[2]);
  const unsigned ro = M_RED + ((x.g & 1) * 32 + 16 * (x.g >> 1) + x.t) * 16 + 4 * x.w;
  *lp<float>(ro) = vg; *lp<float>(ro + 1024) = vu;
  BAR();
  const f32x4 g4 = *lp<f32x4>(M_RED + x.lane * 16), u4 = *lp<f32x4>(M_RED + 1024 + x.lane * 16);
  const float rs = rsq((hi ? f_(sb.y) : f_(sa.y)) + EPS), sg = hi ? key_sig(k2) : key_sig(k1);
  const float gv = ((g4[0] + g4[1]) + (g4[2] + g4[3])) * rs, uv = ((u4[0] + u4[1]) + (u4[2] + u4[3])) * rs;
  const float h = gv * rcp(1.f + ex2(-LOG2E * gv)) * uv * sg;
  *lp<u16>(M_HF + x.w * 128 + x.lane * 2) = (u16)pk(h, h);
  const v4u hf = *lp<v4u>(M_HF + x.w * 128 + ((x.t >> 2) & 1) * 64 + x.g * 16);
  f32x4 y4[8];
#pragma unroll
  for (int c = 0; c < 8; ++c) mf_vv<true>(y4[c], hf, F.WD[c]);
  TIE4(3, y4[0], y4[1], y4[2], y4[3]); TIE4(3, y4[4], y4[5], y4[6], y4[7]);
  float y[8];
#pragma unroll
  for (int c = 0; c < 8; ++c) y[c] = y4[c][0];
  const unsigned yo = hi ? OOB : ((x.g & 1) * 44 + x.ff) * 1024 + x.w * 256 + x.t * 16;
  stc4(x.rS, yo, v4u{pk(y[0], y[1]), pk(y[2], y[3]), pk(y[4], y[5]), pk(y[6], y[7])}, W_Y + L * Y_L);
}
DEV v4u sel_frag(int t) {
  const unsigned v = (t & 1) ? 0x3F800000u : 0x00003F80u;
  return v4u{(t >> 1) == 0 ? v : 0u, (t >> 1) == 1 ? v : 0u, (t >> 1) == 2 ? v : 0u, (t >> 1) == 3 ? v : 0u};
}
DEV void y_slice(const Ctx& x, int L, int slot) {
  const v4u sel = sel_frag(x.t);
  const int p0 = 4 * (x.t >> 2) + x.g;
  const unsigned o = (slot * 44 + p0) * 1024 + (128 * x.q + 32 * x.w) * 2 + (x.t & 3) * 16;
  f32x4 d;
  for (int sp = 0; sp < SPIN; ++sp) {
    MEMF();
    const v4u a = ldc4(x.rS, o, W_Y + L * Y_L), b = ldc4(x.rS, o + 16384, W_Y + L * Y_L), c = ldc4(x.rS, p0 < NFF - 32 ? o + 32768 : OOB, W_Y + L * Y_L);
    mf_vvn<true>(d, sel, a); mf_vvn<false>(d, sel, b); mf_vvn<false>(d, sel, c);
    TIE1(7, d);
    if (__all(max(max(a.x, b.x), c.x) != POISON)) break;
  }
#pragma unroll
  for (int r = 0; r < 4; ++r) { d[r] += dppf<0x124>(d[r]); d[r] += dppf<0x128>(d[r]); }
  if (x.g < 2) *lp<f32x4>(M_YS + x.w * 128 + ((x.t & 3) * 8 + 4 * x.g) * 4) = d;
}

DEV void outproj(Ctx& x, const v4u (&WO)[8][2], const v4u (&ob)[8], f32x4 (&acc)[2][2], const v4u wfr, const float wsum, int L, v4u (&xk)[2]) {
  const int w = x.w;
#pragma unroll
  for (int j = 0; j < 4; ++j) {
    const v4u a = ob[2 * j], b = ob[2 * j + 1];
    *lp<v4u>(XIMG + (4 * w + j) * 1024 + x.lane * 16) = v4u{a.x, a.y, b.x, b.y};
    *lp<v4u>(XIMG + (16 + 4 * w + j) * 1024 + x.lane * 16) = v4u{a.z, a.w, b.z, b.w};
  }
  BAR();
  {
    v4u of[3][2];
#pragma unroll
    for (int j = 0; j < 2; ++j) { of[j][0] = *lp<v4u>(XIMG + j * 1024 + x.lane * 16); of[j][1] = *lp<v4u>(XIMG + (16 + j) * 1024 + x.lane * 16); }
    TIE4(1, acc[0][0], acc[0][1], acc[1][0], acc[1][1]);
    sfor<0, 32>([&](auto J) {
      constexpr int j = J.v >> 1, T = J.v & 1;
      if constexpr (j < 8) mf_av<false, AO(j, 0)>(acc[T][0], of[j % 3][T]); else mf_vv<false>(acc[T][0], WO[j - 8][0], of[j % 3][T]);
      if constexpr (j + 2 < 16) of[(j + 2) % 3][T] = *lp<v4u>(XIMG + (16 * T + j + 2) * 1024 + x.lane * 16);
      SB();
      if constexpr (j < 8) mf_av<false, AO(j, 1)>(acc[T][1], of[j % 3][T]); else mf_vv<false>(acc[T][1], WO[j - 8][1], of[j % 3][T]);
      SB();
    });
    TIE4(7, acc[0][0], acc[0][1], acc[1][0], acc[1][1]);
  }
  STAMP(8 + 12 * L);
#pragma unroll
  for (int T = 0; T < 2; ++T) {
    xk[T] = v4u{pk(acc[T][0][0], acc[T][0][1]), pk(acc[T][0][2], acc[T][0][3]), pk(acc[T][1][0], acc[T][1][1]), pk(acc[T][1][2], acc[T][1][3])};
    stc4(x.rS, x.lane * 16, xk[T], W_X + L * BIG_L + ((4 * x.c2 + 2 * x.u + T) * 16 + 4 * x.q + w) * 1024);
  }
  f32x4 dd[2];
  mf_vvn<true>(dd[0], wfr, xk[0]); mf_vvn<true>(dd[1], wfr, xk[1]);
  TIE2(7, dd[0], dd[1]);
#pragma unroll
  for (int T = 0; T < 2; ++T) {
    const f32x4 d = dd[T];
    float s2 = dot2(xk[T].x, xk[T].x, dot2(xk[T].y, xk[T].y, 0.f)) + dot2(xk[T].z, xk[T].z, dot2(xk[T].w, xk[T].w, 0.f));
    s2 = x32sum(x16sum(s2));
    if (x.g == 0) *lp<f32x4>(M_RED + (w * 32 + 16 * T + x.t) * 16) = f32x4{d[0], d[1], s2, 0.f};
  }
  BAR();
  if (w == 0) {
    const int tk = x.lane & 31, hf = x.lane >> 5;
    const unsigned pb = W_PST + L * PST_L + (x.c2 * 2 + x.u) * 2048;
    {
      const f32x4 a = *lp<f32x4>(M_RED + (hf * 64 + tk) * 16) + *lp<f32x4>(M_RED + (hf * 64 + 32 + tk) * 16);
      const f32x4 s = {x32sum(a[0]), x32sum(a[1]), x32sum(a[2]), 0.f};
      stc4(x.rS, hf ? OOB : x.q * 512 + tk * 16, __builtin_bit_cast(v4u, s), pb);
    }
    DRAIN();
    v4u a, b;
    for (int sp = 0; sp < SPIN; ++sp) {
      MEMF();
      a = ldc4(x.rS, hf * 1024 + tk * 16, pb); b = ldc4(x.rS, hf * 1024 + 512 + tk * 16, pb);
      if (__all(a.w != POISON && b.w != POISON)) break;
    }
    const float s1 = x32sum(f_(a.x) + f_(b.x)), sw = x32sum(f_(a.y) + f_(b.y)), s2 = x32sum(f_(a.z) + f_(b.z));
    const float m = s1 * (1.f / 512), var = fmaxf(s2 * (1.f / 512) - m * m, 0.f), rs = rsq(var + EPS);
    if (x.q == 0) {
      const float r = rs * (sw - m * wsum);
      unsigned k = u_(r); k = (k & 0x80000000u) ? ~k : (k | 0x80000000u);
      const int tok = x.c2 * 64 + x.u * 32 + tk;
      k = (k & ~0x7FFu) | (unsigned)(2047 - tok);
      const unsigned m1 = x16maxu(rowmaxu(k)), m2 = x16maxu(rowmaxu(k == m1 ? 0u : k));
      stc2(x.rS, x.lane == 0 ? (x.c2 * 2 + x.u) * 128 : OOB, v2u{m1, m2}, W_KEY + L * KEY_L);
      stc2(x.rS, hf ? OOB : tok * 8, v2u{u_(m), u_(var)}, W_ST + L * ST_L);
    }
    *lp<f32x2>(M_PS + tk * 8) = f32x2{m, rs};
  }
}
DEV void load_o(const Ctx& x, int L, v4u (&ob)[8]) {
  const unsigned so = W_O + L * BIG_L + ((x.c2 * 2 + x.u) * 32 + 8 * x.w) * 1024;
  for (int sp = 0; sp < SPIN; ++sp) {
    MEMF();
#pragma unroll
    for (int i = 0; i < 8; ++i) ob[i] = ldc4(x.rS, x.lane * 16 + i * 1024, so);
    const unsigned m = max(max(max(ob[0].x, ob[1].x), max(ob[2].x, ob[3].x)), max(max(ob[4].x, ob[5].x), max(ob[6].x, ob[7].x)));
    if (__all(m != POISON)) break;
  }
}
DEV void unpack8(v4u v, f32x4& a, f32x4& b) { a = f32x4{blo(v.x), bhi(v.x), blo(v.y), bhi(v.y)}; b = f32x4{blo(v.z), bhi(v.z), blo(v.w), bhi(v.w)}; }

__global__ void __launch_bounds__(256, 1)
fused(const u16* __restrict__ x0, const u16* __restrict__ wqkv, const u16* __restrict__ wo, const u16* __restrict__ wr, const u16* __restrict__ wg,
      const u16* __restrict__ wu, const u16* __restrict__ wd, u16* __restrict__ out, char* __restrict__ ws) {
  Ctx x;
  const int b = blockIdx.x, tid = threadIdx.x, lane = tid & 63, w = __builtin_amdgcn_readfirstlane(tid >> 6), t = lane & 15, g = lane >> 4;
  const int xc = b & 7, jb = b >> 3;
  x.lane = lane; x.w = w; x.t = t; x.g = g;
  x.c = (xc >> 1) * 8 + (jb >> 2); x.hg = (xc & 1) * 4 + (jb & 3); x.u = xc & 1; x.q = jb & 3;
  x.c2 = MAPPING == 1 ? x.c : ((x.c + 8) & 31);
  { const int f = (jb / 6) * 8 + xc; x.ff = (jb % 6 == 1 && f < NFF) ? f : -1; }
  const int c = x.c, hg = x.hg;
  x.rS = mk_rs(ws, W_END);
  const v4u sS = mk_srd(ws, W_END), sX = mk_srd(x0, 2048 * 1024), sW = mk_srd(wqkv, 512 * 3072), sO = mk_srd(wo, 512 * 1024);
  CLAIM_AGPRS();
  STAMP(0);

  v2u ep;
  asm volatile("buffer_load_dwordx2 %0, off, %1, 0 sc1" : "=v"(ep) : "s"(sS) : "memory");
  sfor<0, 20>([&](auto I) {
    constexpr int i = I.v;
    const int R = 20 * w + i, tok = (c == 0 && R < 16) ? R : 64 * c - 16 + R;
    dma<i * 1024, 0>(sX, (lane ^ (R & 15)) * 16, XIMG + 20 * w * 1024, tok * 1024);
  });
  const unsigned wvo = (8 * w + (lane >> 3)) * 3072 + hg * 128 + (((((lane & 7) >> 1) ^ (((lane >> 4) & 1) + 2 * (w & 1))) * 2) + (lane & 1)) * 16;
  const unsigned ringw = RING + w * 1024;
  auto issue_w = [&](auto S) {
    constexpr int s = S.v;
    sfor<0, 3>([&](auto P) { dma<(s % 6) * 12288 + P.v * 4096, s * 98304 + P.v * 1024>(sW, wvo, ringw, 0); });
  };
  const unsigned ovo = g * 1024 + x.q * 256 + (t ^ (4 * g)) * 16;
  auto issue_o_ring = [&](auto S) {
    constexpr int sl = S.v;
    sfor<0, 3>([&](auto P) { dma<(12 * sl + P.v) * 1024, (12 * sl + P.v) * 4096>(sO, ovo, RING + 3 * w * 1024, 3 * w * 4096); });
  };
  const v4u pz = {POISON, POISON, POISON, POISON};
  sfor<0, 5>([&](auto I) {
    constexpr int i = I.v;
    waitv<16 + i>();
    if constexpr (i < 2) {
      const unsigned ob = W_O + i * BIG_L + (c * 64 + 4 * hg + w) * 1024;
      stc4(x.rS, lane * 16, pz, ob); stc4(x.rS, lane * 16, pz, ob + 32768);
    } else if constexpr (i < 4) {
      const unsigned xb = W_X + (i - 2) * BIG_L + ((4 * x.c2 + 2 * x.u) * 16 + 4 * x.q + w) * 1024;
      stc4(x.rS, lane * 16, pz, xb); stc4(x.rS, lane * 16, pz, xb + 16384);
    } else {
      const int gi = x.c2 * 2 + x.u;
      unsigned so = OOB;
      if (w == 1) so = W_PST + (lane >> 5) * PST_L + gi * 2048 + x.q * 512 + (lane & 31) * 16;
      if (w == 2 && x.q == 0) so = lane < 32 ? W_ST + g * ST_L + gi * 256 + t * 16 : (lane < 34 ? W_KEY + (lane - 32) * KEY_L + gi * 128 : OOB);
      stc4(x.rS, so, pz);
      stc4(x.rS, x.ff >= 0 ? W_Y + (w >> 1) * Y_L + ((w & 1) * 44 + x.ff) * 1024 + lane * 16 : OOB, pz);
    }
    issue_w(I);
    const int R = 20 * w + 4 * i + g;
    const unsigned ra = XIMG + R * 1024 + t * 16;
    float s1a = 0.f, s1b = 0.f, s2a = 0.f, s2b = 0.f;
#pragma unroll
    for (int k = 0; k < 4; ++k) {
      const v4u v = *lp<v4u>(ra + k * 256);
      s1a = dot2(v.x, ONE2, s1a); s2a = dot2(v.x, v.x, s2a); s1b = dot2(v.y, ONE2, s1b); s2b = dot2(v.y, v.y, s2b);
      s1a = dot2(v.z, ONE2, s1a); s2a = dot2(v.z, v.z, s2a); s1b = dot2(v.w, ONE2, s1b); s2b = dot2(v.w, v.w, s2b);
    }
    const float m = rowsum(s1a + s1b) * (1.f / 512), var = fmaxf(rowsum(s2a + s2b) * (1.f / 512) - m * m, 0.f);
    *lp<float>(M_STM + R * 4) = m; *lp<float>(M_STA + R * 4) = rsq(var + EPS);
    if (i == 0 && w == 0) *lp<v4u>(XIMG + lane * 16) = v4u{ONE2, ONE2, ONE2, ONE2};
    SB();
  });
  asm volatile("" : "+v"(ep));
  const unsigned e0 = rfl(ep.x) + 1u, e1 = rfl(ep.y);
  STAMP(1);

  Acc a;
  {
    unsigned xb[4];
#pragma unroll
    for (int k = 0; k < 4; ++k) xb[k] = XIMG + t * 1024 + (((4 * k) ^ (t & 12)) | (g ^ (t & 3))) * 16;
    const int qq = t >> 2;
    const unsigned wb = RING + (8 * g + qq) * 128 + (w ^ ((qq >> 1) + 2 * (g & 1))) * 32 + 8 * (lane & 3);
    const unsigned wb2 = wb + 36864;
    v4u xf[2][5];
    sfor<0, 17>([&](auto S) {
      constexpr int s = S.v;
      if constexpr (s < 16) {
        waitv<(s < 4 ? 20 - 2 * s : 12)>();
        BAR();
      }
      auto mm = [&](auto N) {
        constexpr int n = N.v;
        if constexpr (s > 0) {
          constexpr int i = n / 3, p = n % 3, z = (s - 1) & 1;
          if constexpr (p == 0) mf_av<s == 1, AW(s - 1, 1)>(a.K[i], xf[z][i]);
          if constexpr (p == 1) mf_va<s == 1, AW(s - 1, 2)>(a.V[i], xf[z][i]);
          if constexpr (p == 2) mf_av<s == 1, AW(s - 1, 0)>(a.Q[i], xf[z][i]);
        }
        SB();
      };
      sfor<0, 15>([&](auto N) {
        constexpr int n = N.v;
        mm(N);
        if constexpr (s < 16) {
          if constexpr (n < 3) {
            if constexpr (s + 5 < 16) dma<((s + 5) % 6) * 12288 + n * 4096, (s + 5) * 98304 + n * 1024>(sW, wvo, ringw, 0);
            else dma<(12 * ((s + 5) % 6) + n) * 1024, (12 * ((s + 5) % 6) + n) * 4096>(sO, ovo, RING + 3 * w * 1024, 3 * w * 4096);
          } else if constexpr (n < 6) {
            constexpr int o = (s % 3) * 12288 + (n - 3) * 4096;
            trd_a<AW(s, n - 3), o, o + 512>((s % 6) < 3 ? wb : wb2);
          } else if constexpr (n < 11) {
            xf[s & 1][n - 6] = *lp<v4u>(xb[s & 3] + (n - 6) * 16384 + (s >> 2) * 256);
          } else if constexpr (n == 11 && s == 4) {
            stc2(x.rS, lane == 0 ? W_FLAGS + (b * 4 + w) * 8 : OOB, v2u{e0, e1});
          }
          SB();
        }
      });
      if constexpr (s == 15) {
        BAR();
      }
    });
  }
  TIE3(7, a.Q[4], a.K[4], a.V[4]);
#pragma unroll
  for (int i = 0; i < 4; ++i) asm volatile("" : "+v"(a.Q[i]), "+v"(a.K[i]), "+v"(a.V[i]));
  STAMP(2);
  const rsrc_t rX = mk_rs(x0, 2048 * 1024), rR = mk_rs(wr, 1024);
  const int n0 = 128 * x.q + 32 * w;
  v4u xk[2];
#pragma unroll
  for (int T = 0; T < 2; ++T) xk[T] = ldp4(rX, (16 * T + t) * 1024 + g * 16, (64 * x.c2 + 32 * x.u) * 1024 + n0 * 2);
  const v4u wr8 = ldp4(rR, g * 16, n0 * 2), wrall = ldp4(rR, lane * 16);
  issue_o_ring(IC<3>{});
  sfor<0, 14>([&](auto K) { dma<K.v * 1024, K.v * 4096>(sO, ovo, XIMG + 24576 + 14 * w * 1024, (72 + 14 * w) * 4096); });
  v4u fl0 = ldc4(x.rS, lane * 16, W_FLAGS + w * 2048), fl1 = ldc4(x.rS, lane * 16 + 1024, W_FLAGS + w * 2048);
  SB();
  if (t == 0) { *lp<f32x4>(M_CS + w * 192 + g * 16) = a.Q[0]; *lp<f32x4>(M_CS + w * 192 + 64 + g * 16) = a.K[0]; }
  if (g == 0) *lp<float>(M_CS + w * 192 + 128 + t * 4) = a.V[0][0];
  attention(x, a, 0);
  STAMP(3);
  v4u WO[8][2];
  const unsigned obs = (4 * g + (t >> 2)) * 256 + ((4 * w + (lane & 3)) ^ (t & 12)) * 16;
  waitv<18>();
  BAR();
  sfor<0, 16>([&](auto J) { constexpr int j = J.v >> 1, ct = J.v & 1; trd_a<AO(j, ct), j * 8192 + 8 * ct, j * 8192 + 4096 + 8 * ct>(RING + obs); });
#pragma unroll
  for (int ct = 0; ct < 2; ++ct) WO[0][ct] = trd2(RING + 65536 + obs + 8 * ct, RING + 65536 + obs + 4096 + 8 * ct);
  SB();
  DRAIN();
  for (int sp = 0; sp < SPIN; ++sp) {
    if (__all(fl0.x == e0 && fl0.y == e1 && fl0.z == e0 && fl0.w == e1 && fl1.x == e0 && fl1.y == e1 && fl1.z == e0 && fl1.w == e1)) break;
    MEMF();
    fl0 = ldc4(x.rS, lane * 16, W_FLAGS + w * 2048); fl1 = ldc4(x.rS, lane * 16 + 1024, W_FLAGS + w * 2048);
  }
  BAR();
  STAMP(4);
  if (b == 0 && w == 3) stc2(x.rS, lane == 0 ? W_EPOCH : OOB, v2u{e0, e1});
  v4u ob[8];
  {
    const unsigned so = W_O + ((x.c2 * 2 + x.u) * 32 + 8 * w) * 1024;
#pragma unroll
    for (int i = 0; i < 8; ++i) ob[i] = ldc4(x.rS, lane * 16 + i * 1024, so);
    SB();
#pragma unroll
    for (int j = 9; j < 16; ++j)
#pragma unroll
      for (int ct = 0; ct < 2; ++ct) WO[j - 8][ct] = trd2(XIMG + 24576 + obs + (j - 9) * 8192 + 8 * ct, XIMG + 24576 + obs + (j - 9) * 8192 + 4096 + 8 * ct);
    SB();
    const unsigned m = max(max(max(ob[0].x, ob[1].x), max(ob[2].x, ob[3].x)), max(max(ob[4].x, ob[5].x), max(ob[6].x, ob[7].x)));
    if (!__all(m != POISON)) load_o(x, 0, ob);
  }
  STAMP(5);
  const v4u wfr = t == 0 ? v4u{ONE2, ONE2, ONE2, ONE2} : (t == 1 ? wr8 : v4u{0u, 0u, 0u, 0u});
  const float wsum = x32sum(x16sum(rowsum(dot2(wrall.x, ONE2, dot2(wrall.y, ONE2, 0.f)) + dot2(wrall.z, ONE2, dot2(wrall.w, ONE2, 0.f)))));
  f32x4 acc[2][2];
#pragma unroll
  for (int T = 0; T < 2; ++T) unpack8(xk[T], acc[T][0], acc[T][1]);
  BAR();
  if (x.ff >= 0) {
    const v4u sG = mk_srd(wg, 512 * 2752), sU = mk_srd(wu, 512 * 2752);
    const unsigned gvo = (lane >> 2) * 2752 + x.ff * 64 + (((((lane >> 1) & 1) ^ (lane >> 5)) * 2) + (lane & 1)) * 16;
    sfor<0, 8>([&](auto K) {
      dma<K.v * 1024, K.v * 16 * 2752>(sG, gvo, RING + w * 8192, w * 128 * 2752);
      dma<32768 + K.v * 1024, K.v * 16 * 2752>(sU, gvo, RING + w * 8192, w * 128 * 2752);
    });
  }
  outproj(x, WO, ob, acc, wfr, wsum, 0, xk);
  STAMP(9);
  const rsrc_t rD = mk_rs(wd, 1376 * 1024);
  if (w == 1) {
    DRAIN();
    const int gi = max(2 * c - 1 + (t >> 2), 0);
    const unsigned o = t < 12 ? gi * 2048 + (t & 3) * 512 + 12 : OOB;
    for (int sp = 0; sp < SPIN; ++sp) {
      MEMF();
      if (__all(ldc1(x.rS, o, W_PST) != POISON)) break;
    }
  }
  BAR();
  STAMP(10);

  unsigned k1 = 0, k2 = 0;
  auto issue_x1 = [&]() {
#pragma unroll
    for (int k = 0; k < 5; ++k) {
      const int n = 20 * w + 4 * k;
      dma4_sc1(sS, lane * 16, XIMG + n * 1024, W_X + ((4 * c - 1) * 16 + n + ((c == 0 && n < 16) ? 16 : 0)) * 1024);
    }
  };
  issue_x1();
  if (x.ff >= 0) {
    v4u raw[8]; Ffn F;
    ffn_load_down(x, rD, raw);
    DRAIN();
    ffn_prep(x, raw, F);
    top2(x, 0, k1, k2, false);
    STAMP(11);
    ffn_run(x, F, 0, k1, k2);
    STAMP(12);
  }
#pragma unroll 1
  for (int tries = 0; tries < 64; ++tries) {
    DRAIN();
    BAR();
    v4u xf[2][5];
#pragma unroll
    for (int i = 0; i < 5; ++i) xf[0][i] = *lp<v4u>(XIMG + i * 16384 + lane * 16);
    sfor<0, 16>([&](auto S) {
      constexpr int s = S.v;
      sfor<0, 5>([&](auto I) {
        constexpr int i = I.v;
        mf_av<s == 0, AW(s, 1)>(a.K[i], xf[s & 1][i]);
        if constexpr (s < 15) xf[(s + 1) & 1][i] = *lp<v4u>(XIMG + (i * 16 + s + 1) * 1024 + lane * 16);
        SB();
        mf_va<s == 0, AW(s, 2)>(a.V[i], xf[s & 1][i]);
        if constexpr (i > 0) mf_av<s == 0, AW(s, 0)>(a.Q[i], xf[s & 1][i]);
        SB();
      });
    });
    TIE3(7, a.Q[4], a.K[4], a.V[4]);
#pragma unroll
    for (int i = 1; i < 4; ++i) asm volatile("" : "+v"(a.Q[i]), "+v"(a.K[i]), "+v"(a.V[i]));
    TIE0_2(a.K[0], a.V[0]);
    const float chk = (a.K[0][0] + a.K[1][0]) + (a.K[2][0] + a.K[3][0]) + a.K[4][0];
    if (!__any(isnan_(chk))) break;
    BAR();
    issue_x1();
  }
  STAMP(13);
  if (w == 1) {
    v2u sa, sb;
    const int ta = 64 * c - 16 + lane;
    for (int sp = 0; sp < SPIN; ++sp) {
      MEMF();
      sa = ldc2(x.rS, max(ta, 0) * 8, W_ST); sb = ldc2(x.rS, (64 * c + 48 + t) * 8, W_ST);
      if (__all(sa.x != POISON && sb.x != POISON)) break;
    }
    const float ra = 1.f + rsq(f_(sa.y) + EPS), rb = 1.f + rsq(f_(sb.y) + EPS);
    *lp<float>(M_STM + lane * 4) = f_(sa.x); *lp<float>(M_STA + lane * 4) = ra * rsq(f_(sa.y) * ra * ra + EPS);
    *lp<float>(M_STM + (64 + t) * 4) = f_(sb.x); *lp<float>(M_STA + (64 + t) * 4) = rb * rsq(f_(sb.y) * rb * rb + EPS);
  }
  if (x.ff < 0) top2(x, 0, k1, k2, false); else BAR();
  STAMP(14);
#pragma unroll 1
  for (int sl = 0; sl < 2; ++sl) {
    const int tau = key_tok(sl ? k2 : k1), R = tau - (64 * c - 16);
    if (R < 2 || R >= 80 || (c == 0 && R < 16)) continue;
    const v4u sel = sel_frag(t);
    const unsigned o = (sl * 44 + g) * 1024 + w * 256 + t * 16;
    const v2u xr = *lp<v2u>(XIMG + ((R >> 4) * 16 + 4 * w + (t >> 2)) * 1024 + ((t & 3) * 16 + (R & 15)) * 16 + (g & 1) * 8);
    const v2u st = ldc2(x.rS, tau * 8, W_ST);
    f32x4 d;
    for (int sp = 0; sp < SPIN; ++sp) {
      MEMF();
      v4u y[11];
#pragma unroll
      for (int i = 0; i < 11; ++i) y[i] = ldc4(x.rS, (i == 10 && g == 3) ? OOB : o + i * 4096, W_Y);
      unsigned m = 0;
#pragma unroll
      for (int i = 0; i < 11; ++i) { if (i == 0) mf_vvn<true>(d, sel, y[i]); else mf_vv<false>(d, sel, y[i]); m = max(m, y[i].x); }
      TIE1(7, d);
      if (__all(m != POISON)) break;
    }
    STAMP(15);
    const float m1 = f_(st.x), rs1 = rsq(f_(st.y) + EPS);
    const f32x4 xv = {blo(xr.x), bhi(xr.x), blo(xr.y), bhi(xr.y)};
    const f32x4 x2 = xv * (1.f + rs1) - m1 * rs1 + d;
    const v2u xp = {pk(x2[0], x2[1]), pk(x2[2], x2[3])};
    float s1 = dot2(xp.x, ONE2, dot2(xp.y, ONE2, 0.f)), s2 = dot2(xp.x, xp.x, dot2(xp.y, xp.y, 0.f));
    s1 = x16sum(rowsum(s1)); s2 = x16sum(rowsum(s2));
    if (g < 2) { *lp<v2u>(M_FIX + w * 256 + t * 16 + g * 8) = xp; *lp<f32x2>(M_FST + w * 8) = f32x2{s1, s2}; }
    BAR();
    f32x4 fq, fk, fv;
    {
      v4u xf[4];
      const unsigned fb = M_FIX + g * 16;
#pragma unroll
      for (int s = 0; s < 3; ++s) xf[s] = *lp<v4u>(fb + s * 64);
      sfor<0, 16>([&](auto S) {
        constexpr int s = S.v;
        mf_av<s == 0, AW(s, 0)>(fq, xf[s & 3]);
        if constexpr (s + 3 < 16) xf[(s + 3) & 3] = *lp<v4u>(fb + (s + 3) * 64);
        SB();
        mf_av<s == 0, AW(s, 1)>(fk, xf[s & 3]); mf_va<s == 0, AW(s, 2)>(fv, xf[s & 3]);
        SB();
      });
      TIE3(7, fq, fk, fv);
    }
    const f32x4 sa = *lp<f32x4>(M_FST), sb = *lp<f32x4>(M_FST + 16);
    const float m2 = ((sa[0] + sa[2]) + (sb[0] + sb[2])) * (1.f / 512);
    const float v2 = fmaxf(((sa[1] + sa[3]) + (sb[1] + sb[3])) * (1.f / 512) - m2 * m2, 0.f);
    const bool mt = t == (R & 15);
#pragma unroll
    for (int i = 0; i < 5; ++i)
      if ((R >> 4) == i) {
#pragma unroll
        for (int r = 0; r < 4; ++r) {
          if (i > 0) a.Q[i][r] = mt ? fq[r] : a.Q[i][r];
          a.K[i][r] = mt ? fk[r] : a.K[i][r];
          a.V[i][r] = (4 * g + r == (R & 15)) ? fv[r] : a.V[i][r];
        }
      }
    *lp<float>(M_STM + R * 4) = m2; *lp<float>(M_STA + R * 4) = rsq(v2 + EPS);
    BAR();
  }
  STAMP(16);
  attention(x, a, 1);
  STAMP(17);
#pragma unroll
  for (int T = 0; T < 2; ++T) {
    const f32x2 ps = *lp<f32x2>(M_PS + (16 * T + t) * 8);
    unpack8(xk[T], acc[T][0], acc[T][1]);
    acc[T][0] = acc[T][0] * (1.f + ps[1]) - ps[0] * ps[1]; acc[T][1] = acc[T][1] * (1.f + ps[1]) - ps[0] * ps[1];
  }
#pragma unroll 1
  for (int sl = 0; sl < 2; ++sl) {
    const int R = key_tok(sl ? k2 : k1) - (64 * x.c2 + 32 * x.u);
    if (R < 0 || R >= 32) continue;
    y_slice(x, 0, sl);
    const f32x4 ya = *lp<f32x4>(M_YS + w * 128 + g * 32), yb = *lp<f32x4>(M_YS + w * 128 + g * 32 + 16);
#pragma unroll
    for (int T = 0; T < 2; ++T) { const float f = (16 * T + t == R) ? 1.f : 0.f; acc[T][0] += ya * f; acc[T][1] += yb * f; }
  }
  DRAIN();
  STAMP(18);
  load_o(x, 1, ob);
  STAMP(19);
  BAR();
  outproj(x, WO, ob, acc, wfr, wsum, 1, xk);
  STAMP(21);
  BAR();
  const rsrc_t rOut = mk_rs(out, 2048 * 1024);
  const unsigned oso = (64 * x.c2 + 32 * x.u) * 1024 + n0 * 2;
#pragma unroll
  for (int T = 0; T < 2; ++T) {
    const f32x2 ps = *lp<f32x2>(M_PS + (16 * T + t) * 8);
    unpack8(xk[T], acc[T][0], acc[T][1]);
    acc[T][0] = acc[T][0] * (1.f + ps[1]) - ps[0] * ps[1]; acc[T][1] = acc[T][1] * (1.f + ps[1]) - ps[0] * ps[1];
    __builtin_amdgcn_raw_buffer_store_b128(v4u{pk(acc[T][0][0], acc[T][0][1]), pk(acc[T][0][2], acc[T][0][3]), pk(acc[T][1][0], acc[T][1][1]), pk(acc[T][1][2], acc[T][1][3])},
                                           rOut, (16 * T + t) * 1024 + g * 16, oso, 0);
  }
  STAMP(22);
  if (x.ff >= 0) {
    v4u raw[8]; Ffn F;
    ffn_load_down(x, rD, raw);
    ffn_prep(x, raw, F);
    top2(x, 1, k1, k2, false);
    STAMP(23);
    ffn_run(x, F, 1, k1, k2);
    STAMP(24);
  } else top2(x, 1, k1, k2, true);
  STAMP(25);
#pragma unroll 1
  for (int sl = 0; sl < 2; ++sl) {
    const int R = key_tok(sl ? k2 : k1) - (64 * x.c2 + 32 * x.u);
    if (R < 0 || R >= 32) continue;
    y_slice(x, 1, sl);
    const f32x4 ya = *lp<f32x4>(M_YS + w * 128 + g * 32), yb = *lp<f32x4>(M_YS + w * 128 + g * 32 + 16);
    const f32x4 a0 = (R < 16 ? acc[0][0] : acc[1][0]) + ya, a1 = (R < 16 ? acc[0][1] : acc[1][1]) + yb;
    __builtin_amdgcn_raw_buffer_store_b128(v4u{pk(a0[0], a0[1]), pk(a0[2], a0[3]), pk(a1[0], a1[1]), pk(a1[2], a1[3])},
                                           rOut, t == (R & 15) ? R * 1024 + g * 16 : OOB, oso, 0);
  }
  STAMP(26);
}
}

extern "C" void kernel_launch(void* const* d_in, const int* in_sizes, int n_in, void* d_out, int out_size, void* d_ws, size_t ws_size, hipStream_t stream) {
  fused<<<256, 256, 0, stream>>>((const u16*)d_in[0], (const u16*)d_in[1], (const u16*)d_in[2], (const u16*)d_in[4], (const u16*)d_in[5],
                                 (const u16*)d_in[6], (const u16*)d_in[7], (u16*)d_out, (char*)d_ws);
}
```

```cpp
#include <hip/hip_runtime.h>

namespace {
typedef unsigned short u16;
typedef unsigned long long u64;
typedef unsigned v4u __attribute__((ext_vector_type(4)));
typedef unsigned v2u __attribute__((ext_vector_type(2)));
typedef float f32x4 __attribute__((ext_vector_type(4)));
typedef float f32x2 __attribute__((ext_vector_type(2)));
typedef __bf16 bf16x8 __attribute__((ext_vector_type(8)));
typedef __bf16 bf16x2 __attribute__((ext_vector_type(2)));
typedef short s16x4 __attribute__((ext_vector_type(4)));
typedef __amdgpu_buffer_rsrc_t rsrc_t;
#define LAS __attribute__((address_space(3)))
#define DEV __device__ __forceinline__

#define MAPPING 0

constexpr int W_EPOCH = 0, W_FLAGS = 4096, W_KEY = 16384, W_ST = 32768, W_PST = 65536, W_Y = 393216;
constexpr int KEY_L = 8192, ST_L = 16384, PST_L = 131072, Y_L = 98304;
constexpr int W_O = 1 << 20, W_X = 5 << 20, BIG_L = 2 << 20, W_END = 9 << 20;
constexpr int W_DBG = 16 << 20;
constexpr unsigned OOB = 0x7ff00000u;
constexpr unsigned POISON = 0xFFFFFFFFu;
constexpr int NFF = 43;
constexpr int SPIN = 1 << 15;
constexpr float EPS = 1e-5f;
constexpr int XIMG = 0, RING = 81920, MISC = 155648, LDS_BYTES = 163840;
constexpr int M_STM = MISC, M_STA = MISC + 320, M_RED = MISC + 1024, M_PS = MISC + 3072, M_TOP = MISC + 3328, M_HF = MISC + 3584,
              M_FIX = MISC + 4096, M_FST = MISC + 5120, M_YS = MISC + 5376, M_DBG = MISC + 6144, M_CS = MISC + 7168;

__shared__ __attribute__((aligned(16))) char smem[LDS_BYTES];
template <class T> DEV LAS T* lp(unsigned off) { return (LAS T*)((LAS char*)smem + off); }

#define MEMF() asm volatile("" ::: "memory")
#define WAITV(n) asm volatile("s_waitcnt vmcnt(" #n ")" ::: "memory")
#define WAITL() asm volatile("s_waitcnt lgkmcnt(0)" ::: "memory")
#define DRAIN() WAITV(0)
#define BAR() do { WAITL(); __builtin_amdgcn_s_barrier(); MEMF(); } while (0)
#define SB() __builtin_amdgcn_sched_barrier(0)
template <int N> DEV void waitv() { asm volatile("s_waitcnt vmcnt(%0)" ::"n"(N) : "memory"); }

#define STAMP(i)

DEV v4u mk_srd(const void* p, unsigned bytes) { const u64 a = (u64)p; return v4u{(unsigned)a, (unsigned)(a >> 32) & 0xffffu, bytes, 0x00020000u}; }
DEV rsrc_t mk_rs(const void* p, unsigned bytes) { return __builtin_amdgcn_make_buffer_rsrc((void*)p, 0, bytes, 0x00020000); }
template <int LI, int SI> DEV void dma(v4u srd, unsigned vo, unsigned lb, unsigned sb) {
  unsigned t;
  asm volatile("s_add_i32 m0, %3, %5\n\ts_add_i32 %0, %4, %6\n\tbuffer_load_dwordx4 %1, %2, %0 offen lds"
               : "=&s"(t) : "v"(vo), "s"(srd), "s"(lb), "s"(sb), "n"(LI), "n"(SI) : "memory", "scc");
}
DEV void dma4_sc1(v4u srd, unsigned vo, unsigned lb, unsigned sb) {
  asm volatile("s_mov_b32 m0, %2\n\ts_nop 0\n\tbuffer_load_dwordx4 %0, %1, %3 offen sc1 lds\n\tbuffer_load_dwordx4 %0, %1, %3 offen offset:1024 sc1 lds\n\t"
               "buffer_load_dwordx4 %0, %1, %3 offen offset:2048 sc1 lds\n\tbuffer_load_dwordx4 %0, %1, %3 offen offset:3072 sc1 lds"
               ::"v"(vo), "s"(srd), "s"(lb), "s"(sb) : "memory");
}
DEV v4u ldc4(rsrc_t r, unsigned vo, unsigned so = 0) { return __builtin_amdgcn_raw_buffer_load_b128(r, vo, so, 16); }
DEV v2u ldc2(rsrc_t r, unsigned vo, unsigned so = 0) { return __builtin_amdgcn_raw_buffer_load_b64(r, vo, so, 16); }
DEV unsigned ldc1(rsrc_t r, unsigned vo, unsigned so = 0) { return __builtin_amdgcn_raw_buffer_load_b32(r, vo, so, 16); }
DEV void stc4(rsrc_t r, unsigned vo, v4u v, unsigned so = 0) { __builtin_amdgcn_raw_buffer_store_b128(v, r, vo, so, 16); }
DEV void stc2(rsrc_t r, unsigned vo, v2u v, unsigned so = 0) { __builtin_amdgcn_raw_buffer_store_b64(v, r, vo, so, 16); }
DEV v4u ldp4(rsrc_t r, unsigned vo, unsigned so = 0) { return __builtin_amdgcn_raw_buffer_load_b128(r, vo, so, 0); }

#define MF_(op, pre, d, a, b, ca, cb) asm volatile(pre op " %0, %1, %2, %0" : "+v"(d) : ca(a), cb(b))
#define MFZ_(op, pre, d, a, b, ca, cb) asm volatile(pre op " %0, %1, %2, 0" : "=&v"(d) : ca(a), cb(b))
#define MFC_(op, pre, d, a, b, c) asm volatile(pre op " %0, %1, %2, %3" : "=&v"(d) : "v"(a), "v"(b), "v"(c))
#define M32 "v_mfma_f32_16x16x32_bf16"
#define M16 "v_mfma_f32_16x16x16_bf16"
#define NOP2 "s_nop 1\n\t"
constexpr int AW(int s, int p) { return (s * 3 + p) * 4; }
constexpr int AO(int j, int ct) { return 192 + (j * 2 + ct) * 4; }
template <bool Z, int A> DEV void mf_av(f32x4& d, const v4u& b) {
  if constexpr (Z) asm volatile(M32 " %0, a[%c2:%c3], %1, 0" : "=&v"(d) : "v"(b), "n"(A), "n"(A + 3));
  else asm volatile(M32 " %0, a[%c2:%c3], %1, %0" : "+v"(d) : "v"(b), "n"(A), "n"(A + 3));
}
template <bool Z, int A> DEV void mf_va(f32x4& d, const v4u& b) {
  if constexpr (Z) asm volatile(M32 " %0, %1, a[%c2:%c3], 0" : "=&v"(d) : "v"(b), "n"(A), "n"(A + 3));
  else asm volatile(M32 " %0, %1, a[%c2:%c3], %0" : "+v"(d) : "v"(b), "n"(A), "n"(A + 3));
}
template <int A, int O0, int O1> DEV void trd_a(unsigned base) {
  static_assert(O0 < 65536 && O1 < 65536, "DS offset");
  asm volatile("ds_read_b64_tr_b16 a[%c1:%c2], %0 offset:%c5\n\tds_read_b64_tr_b16 a[%c3:%c4], %0 offset:%c6"
               ::"v"(base), "n"(A), "n"(A + 1), "n"(A + 2), "n"(A + 3), "n"(O0), "n"(O1) : "memory");
}
#define A8(n) "a" #n "0", "a" #n "1", "a" #n "2", "a" #n "3", "a" #n "4", "a" #n "5", "a" #n "6", "a" #n "7", "a" #n "8", "a" #n "9"
#define CLAIM_AGPRS() asm volatile("" ::: A8(), A8(1), A8(2), A8(3), A8(4), A8(5), A8(6), A8(7), A8(8), A8(9), A8(10), A8(11), A8(12), A8(13), A8(14), A8(15), A8(16), \
    A8(17), A8(18), A8(19), A8(20), A8(21), A8(22), A8(23), A8(24), "a250", "a251", "a252", "a253", "a254", "a255")
template <bool Z> DEV void mf_vv(f32x4& d, const v4u& a, const v4u& b) { if constexpr (Z) MFZ_(M32, "", d, a, b, "v", "v"); else MF_(M32, "", d, a, b, "v", "v"); }
template <bool Z> DEV void mf_vvn(f32x4& d, const v4u& a, const v4u& b) { if constexpr (Z) MFZ_(M32, NOP2, d, a, b, "v", "v"); else MF_(M32, NOP2, d, a, b, "v", "v"); }
#define TIE1(n, a) asm volatile("s_nop " #n : "+v"(a))
#define TIE2(n, a, b) asm volatile("s_nop " #n : "+v"(a), "+v"(b))
#define TIE3(n, a, b, c) asm volatile("s_nop " #n : "+v"(a), "+v"(b), "+v"(c))
#define TIE4(n, a, b, c, d) asm volatile("s_nop " #n : "+v"(a), "+v"(b), "+v"(c), "+v"(d))
#define TIE0_2(a, b) asm volatile("" : "+v"(a), "+v"(b))
#define TIE0_4(a, b, c, d) asm volatile("" : "+v"(a), "+v"(b), "+v"(c), "+v"(d))
DEV v2u trd(unsigned off) { return __builtin_bit_cast(v2u, __builtin_amdgcn_ds_read_tr16_b64_v4i16(lp<s16x4>(off))); }
DEV v4u trd2(unsigned o0, unsigned o1) { const v2u a = trd(o0), b = trd(o1); return v4u{a.x, a.y, b.x, b.y}; }
DEV unsigned pk(float a, float b) { return __builtin_bit_cast(unsigned, __builtin_convertvector((f32x2{a, b}), bf16x2)); }
DEV float blo(unsigned v) { return __builtin_bit_cast(float, v << 16); }
DEV float bhi(unsigned v) { return __builtin_bit_cast(float, v & 0xffff0000u); }
DEV float dot2(unsigned a, unsigned b, float c) { return __builtin_amdgcn_fdot2_f32_bf16(__builtin_bit_cast(bf16x2, a), __builtin_bit_cast(bf16x2, b), c, false); }
DEV float f_(unsigned v) { return __builtin_bit_cast(float, v); }
DEV unsigned u_(float v) { return __builtin_bit_cast(unsigned, v); }
template <int C> DEV unsigned dppu(unsigned v) { return (unsigned)__builtin_amdgcn_update_dpp(0, (int)v, C, 0xF, 0xF, true); }
template <int C> DEV float dppf(float v) { return f_(dppu<C>(u_(v))); }
DEV float rowsum(float v) { v += dppf<0x128>(v); v += dppf<0x124>(v); v += dppf<0x122>(v); v += dppf<0x121>(v); return v; }
DEV unsigned rowmaxu(unsigned v) { v = max(v, dppu<0x128>(v)); v = max(v, dppu<0x124>(v)); v = max(v, dppu<0x122>(v)); v = max(v, dppu<0x121>(v)); return v; }
DEV float x16sum(float v) { auto r = __builtin_amdgcn_permlane16_swap(u_(v), u_(v), false, false); return f_(r[0]) + f_(r[1]); }
DEV float x32sum(float v) { auto r = __builtin_amdgcn_permlane32_swap(u_(v), u_(v), false, false); return f_(r[0]) + f_(r[1]); }
DEV float x16max(float v) { auto r = __builtin_amdgcn_permlane16_swap(u_(v), u_(v), false, false); return fmaxf(f_(r[0]), f_(r[1])); }
DEV float x32max(float v) { auto r = __builtin_amdgcn_permlane32_swap(u_(v), u_(v), false, false); return fmaxf(f_(r[0]), f_(r[1])); }
DEV unsigned x16maxu(unsigned v) { auto r = __builtin_amdgcn_permlane16_swap(v, v, false, false); return max((unsigned)r[0], (unsigned)r[1]); }
DEV unsigned x32maxu(unsigned v) { auto r = __builtin_amdgcn_permlane32_swap(v, v, false, false); return max((unsigned)r[0], (unsigned)r[1]); }
DEV unsigned rfl(unsigned v) { return (unsigned)__builtin_amdgcn_readfirstlane((int)v); }
DEV float rsq(float v) { return __builtin_amdgcn_rsqf(v); }
DEV float rcp(float v) { return __builtin_amdgcn_rcpf(v); }
DEV float ex2(float v) { return __builtin_amdgcn_exp2f(v); }
DEV bool isnan_(float v) { return v != v; }
constexpr unsigned ONE2 = 0x3F803F80u;
constexpr float LOG2E = 1.4426950408889634f;

template <int S> struct IC { static constexpr int v = S; };
template <int A, int B, class F> DEV void sfor(F&& f) { if constexpr (A < B) { f(IC<A>{}); sfor<A + 1, B>(f); } }

struct Ctx {
  int lane, w, t, g, c, hg, c2, u, q, ff;
  rsrc_t rS;
  DEV void fresh() { asm volatile("" : "+v"(lane)); t = lane & 15; g = lane >> 4; }
};

struct Acc { f32x4 Q[5], K[5], V[5]; };

DEV void attention(Ctx& x, const Acc& a, int L) {
  x.fresh();
  const int t = x.t, g = x.g;
  const f32x4 csq = *lp<f32x4>(M_CS + x.w * 192 + g * 16), csk = *lp<f32x4>(M_CS + x.w * 192 + 64 + g * 16);
  const float csv = *lp<float>(M_CS + x.w * 192 + 128 + t * 4);
  v2u Qp[5], Kp[5], Vp[5];
  float cq[5];
#pragma unroll
  for (int i = 0; i < 5; ++i) {
    const float m = *lp<float>(M_STM + (16 * i + t) * 4), s = *lp<float>(M_STA + (16 * i + t) * 4);
    const f32x4 m4 = *lp<f32x4>(M_STM + (16 * i + 4 * g) * 4), s4 = *lp<f32x4>(M_STA + (16 * i + 4 * g) * 4);
    const f32x4 k = (a.K[i] - m * csk) * s;
    Kp[i] = v2u{pk(k[0], k[1]), pk(k[2], k[3])};
    const f32x4 v = (a.V[i] - m4 * csv) * s4;
    Vp[i] = v2u{pk(v[0], v[1]), pk(v[2], v[3])};
    if (i > 0) { const f32x4 qq = a.Q[i] - m * csq; Qp[i] = v2u{pk(qq[0], qq[1]), pk(qq[2], qq[3])}; cq[i] = s * (0.25f * LOG2E); }
  }
  f32x4 bp, bc;
#pragma unroll
  for (int r = 0; r < 4; ++r) {
    const int kk = 4 * g + r; const bool par = ((t ^ r) & 1) == 0;
    bc[r] = (par && kk <= t) ? 0.f : -1e30f;
    bp[r] = (par && kk >= t + 2) ? 0.f : -1e30f;
  }
  const v4u ones = {ONE2, ONE2, ONE2, ONE2};
  f32x4 b0 = bp;
  if (x.c == 0) b0 = f32x4{-1e30f, -1e30f, -1e30f, -1e30f};
  f32x4 sp[5], sc[5], o[5], l[5];
#pragma unroll
  for (int i = 1; i < 5; ++i) {
    if (i == 1) MFC_(M16, NOP2, sp[i], Kp[i - 1], Qp[i], b0); else MFC_(M16, NOP2, sp[i], Kp[i - 1], Qp[i], bp);
    MFC_(M16, NOP2, sc[i], Kp[i], Qp[i], bc);
  }
  v2u Op[5];
#pragma unroll
  for (int i = 1; i < 5; ++i) {
    TIE0_2(sp[i], sc[i]);
    float mx = fmaxf(fmaxf(fmaxf(sp[i][0], sp[i][1]), fmaxf(sp[i][2], sp[i][3])), fmaxf(fmaxf(sc[i][0], sc[i][1]), fmaxf(sc[i][2], sc[i][3])));
    mx = x32max(x16max(mx));
    const float cm = -cq[i] * mx;
    const f32x4 ep = sp[i] * cq[i] + cm, ec = sc[i] * cq[i] + cm;
    const v4u P = {pk(ex2(ep[0]), ex2(ep[1])), pk(ex2(ep[2]), ex2(ep[3])), pk(ex2(ec[0]), ex2(ec[1])), pk(ex2(ec[2]), ex2(ec[3]))};
    const v4u VV = {Vp[i - 1].x, Vp[i - 1].y, Vp[i].x, Vp[i].y};
    mf_vvn<true>(l[i], ones, P);
    mf_vvn<true>(o[i], VV, P);
    if (i > 1) {
      TIE0_2(o[i - 1], l[i - 1]);
      const f32x4 on = o[i - 1] * rcp(l[i - 1][0]);
      Op[i - 1] = v2u{pk(on[0], on[1]), pk(on[2], on[3])};
    }
  }
  TIE2(7, o[4], l[4]);
  { const f32x4 on = o[4] * rcp(l[4][0]); Op[4] = v2u{pk(on[0], on[1]), pk(on[2], on[3])}; }
  const unsigned ob = W_O + L * BIG_L + (x.c * 64 + 4 * x.hg + x.w) * 1024;
  stc4(x.rS, x.lane * 16, v4u{Op[1].x, Op[1].y, Op[2].x, Op[2].y}, ob);
  stc4(x.rS, x.lane * 16, v4u{Op[3].x, Op[3].y, Op[4].x, Op[4].y}, ob + 32 * 1024);
}

DEV void top2(const Ctx& x, int L, unsigned& k1, unsigned& k2, bool nap) {
  if (x.w == 0) {
    v2u k;
    for (int sp = 0; sp < SPIN; ++sp) {
      MEMF();
      k = ldc2(x.rS, x.lane * 128, W_KEY + L * KEY_L);
      if (__all(k.x != POISON && k.y != POISON)) break;
      if (nap) __builtin_amdgcn_s_sleep(4);
    }
    const unsigned m1 = x32maxu(x16maxu(rowmaxu(k.x)));
    const unsigned m2 = x32maxu(x16maxu(rowmaxu(k.x == m1 ? k.y : k.x)));
    *lp<v2u>(M_TOP) = v2u{m1, m2};
  }
  BAR();
  const v2u k = *lp<v2u>(M_TOP);
  k1 = rfl(k.x); k2 = rfl(k.y);
}
DEV int key_tok(unsigned k) { return 2047 - (int)(k & 0x7FFu); }
DEV float key_sig(unsigned k) {
  unsigned u = (k & ~0x7FFu) | 0x400u;
  u = (u & 0x80000000u) ? (u ^ 0x80000000u) : ~u;
  return rcp(1.f + ex2(-LOG2E * f_(u)));
}

struct Ffn { v4u GU[4][4]; v4u WD[8]; float cs[4]; };
DEV void ffn_load_down(const Ctx& x, rsrc_t rD, v4u (&raw)[8]) {
#pragma unroll
  for (int e = 0; e < 8; ++e) raw[e] = ldp4(rD, (8 * x.g + e) * 1024 + x.t * 16, x.ff * 32768 + x.w * 256);
}
DEV void ffn_prep(const Ctx& x, const v4u (&raw)[8], Ffn& F) {
  const int qq = x.t >> 2, p = x.lane & 3;
  const unsigned b0 = RING + (128 * x.w + 8 * x.g + qq) * 64 + (x.g & 1) * 32 + 8 * p, b1 = b0 ^ 32;
  const v4u ones = {ONE2, ONE2, ONE2, ONE2};
#pragma unroll
  for (int c = 0; c < 8; ++c)
#pragma unroll
    for (int d = 0; d < 4; ++d) F.WD[c][d] = __builtin_amdgcn_perm(raw[2 * d + 1][c >> 1], raw[2 * d][c >> 1], (c & 1) ? 0x07060302u : 0x05040100u);
  TIE0_4(F.WD[0], F.WD[1], F.WD[2], F.WD[3]); TIE0_4(F.WD[4], F.WD[5], F.WD[6], F.WD[7]);
  SB();
  f32x4 cs[4];
#pragma unroll
  for (int s = 0; s < 4; ++s)
#pragma unroll
    for (int m = 0; m < 4; ++m) {
      const unsigned o = ((m & 1) ? b1 : b0) + s * 2048 + (m >> 1) * 32768;
      F.GU[s][m] = trd2(o, o + 256);
    }
#pragma unroll
  for (int s = 0; s < 4; ++s)
#pragma unroll
    for (int m = 0; m < 4; ++m) { if (s == 0) mf_vvn<true>(cs[m], ones, F.GU[s][m]); else mf_vv<false>(cs[m], ones, F.GU[s][m]); }
  TIE4(7, cs[0], cs[1], cs[2], cs[3]);
#pragma unroll
  for (int m = 0; m < 4; ++m) F.cs[m] = cs[m][0];
}
DEV void ffn_run(const Ctx& x, const Ffn& F, int L, unsigned k1, unsigned k2) {
  const int ta = key_tok(k1), tb = key_tok(k2);
  const int tk = ((x.t >> 2) & 1) ? tb : ta;
  const unsigned xo = ((tk >> 4) * 16 + 4 * x.w) * 1024 + (16 * x.g + (tk & 15)) * 16;
  v4u xf[4]; v2u sa, sb;
  for (int sp = 0; sp < SPIN; ++sp) {
    MEMF();
#pragma unroll
    for (int s = 0; s < 4; ++s) xf[s] = ldc4(x.rS, xo + s * 1024, W_X + L * BIG_L);
    sa = ldc2(x.rS, ta * 8, W_ST + L * ST_L); sb = ldc2(x.rS, tb * 8, W_ST + L * ST_L);
    const unsigned m = max(max(max(xf[0].x, xf[1].x), max(xf[2].x, xf[3].x)), max(sa.x, sb.x));
    if (__all(m != POISON)) break;
  }
  f32x4 d[4];
#pragma unroll
  for (int s = 0; s < 4; ++s)
#pragma unroll
    for (int m = 0; m < 4; ++m) { if (s == 0) mf_vvn<true>(d[m], xf[s], F.GU[s][m]); else mf_vv<false>(d[m], xf[s], F.GU[s][m]); }
  TIE4(7, d[0], d[1], d[2], d[3]);
  const float mt = (x.g & 1) ? f_(sb.x) : f_(sa.x);
  const bool hi = x.g >= 2;
  const float vg = (hi ? d[1][0] : d[0][0]) - mt * (hi ? F.cs[1] : F.cs[0]);
  const float vu = (hi ? d[3][0] : d[2][0]) - mt * (hi ? F.cs[3] : F.cs[2]);
  const unsigned ro = M_RED + ((x.g & 1) * 32 + 16 * (x.g >> 1) + x.t) * 16 + 4 * x.w;
  *lp<float>(ro) = vg; *lp<float>(ro + 1024) = vu;
  BAR();
  const f32x4 g4 = *lp<f32x4>(M_RED + x.lane * 16), u4 = *lp<f32x4>(M_RED + 1024 + x.lane * 16);
  const float rs = rsq((hi ? f_(sb.y) : f_(sa.y)) + EPS), sg = hi ? key_sig(k2) : key_sig(k1);
  const float gv = ((g4[0] + g4[1]) + (g4[2] + g4[3])) * rs, uv = ((u4[0] + u4[1]) + (u4[2] + u4[3])) * rs;
  const float h = gv * rcp(1.f + ex2(-LOG2E * gv)) * uv * sg;
  *lp<u16>(M_HF + x.w * 128 + x.lane * 2) = (u16)pk(h, h);
  const v4u hf = *lp<v4u>(M_HF + x.w * 128 + ((x.t >> 2) & 1) * 64 + x.g * 16);
  f32x4 y4[8];
#pragma unroll
  for (int c = 0; c < 8; ++c) mf_vv<true>(y4[c], hf, F.WD[c]);
  TIE4(3, y4[0], y4[1], y4[2], y4[3]); TIE4(3, y4[4], y4[5], y4[6], y4[7]);
  float y[8];
#pragma unroll
  for (int c = 0; c < 8; ++c) y[c] = y4[c][0];
  const unsigned yo = hi ? OOB : ((x.g & 1) * 44 + x.ff) * 1024 + x.w * 256 + x.t * 16;
  stc4(x.rS, yo, v4u{pk(y[0], y[1]), pk(y[2], y[3]), pk(y[4], y[5]), pk(y[6], y[7])}, W_Y + L * Y_L);
}
DEV v4u sel_frag(int t) {
  const unsigned v = (t & 1) ? 0x3F800000u : 0x00003F80u;
  return v4u{(t >> 1) == 0 ? v : 0u, (t >> 1) == 1 ? v : 0u, (t >> 1) == 2 ? v : 0u, (t >> 1) == 3 ? v : 0u};
}
DEV void y_slice(const Ctx& x, int L, int slot) {
  const v4u sel = sel_frag(x.t);
  const int p0 = 4 * (x.t >> 2) + x.g;
  const unsigned o = (slot * 44 + p0) * 1024 + (128 * x.q + 32 * x.w) * 2 + (x.t & 3) * 16;
  f32x4 d;
  for (int sp = 0; sp < SPIN; ++sp) {
    MEMF();
    const v4u a = ldc4(x.rS, o, W_Y + L * Y_L), b = ldc4(x.rS, o + 16384, W_Y + L * Y_L), c = ldc4(x.rS, p0 < NFF - 32 ? o + 32768 : OOB, W_Y + L * Y_L);
    mf_vvn<true>(d, sel, a); mf_vvn<false>(d, sel, b); mf_vvn<false>(d, sel, c);
    TIE1(7, d);
    if (__all(max(max(a.x, b.x), c.x) != POISON)) break;
  }
#pragma unroll
  for (int r = 0; r < 4; ++r) { d[r] += dppf<0x124>(d[r]); d[r] += dppf<0x128>(d[r]); }
  if (x.g < 2) *lp<f32x4>(M_YS + x.w * 128 + ((x.t & 3) * 8 + 4 * x.g) * 4) = d;
}

DEV void outproj(Ctx& x, const v4u (&WO)[8][2], const v4u (&ob)[8], f32x4 (&acc)[2][2], const v4u wfr, const float wsum, int L, v4u (&xk)[2]) {
  const int w = x.w;
#pragma unroll
  for (int j = 0; j < 4; ++j) {
    const v4u a = ob[2 * j], b = ob[2 * j + 1];
    *lp<v4u>(XIMG + (4 * w + j) * 1024 + x.lane * 16) = v4u{a.x, a.y, b.x, b.y};
    *lp<v4u>(XIMG + (16 + 4 * w + j) * 1024 + x.lane * 16) = v4u{a.z, a.w, b.z, b.w};
  }
  BAR();
  STAMP(7 + 20 * L);
  {
    v4u of[3][2];
#pragma unroll
    for (int j = 0; j < 2; ++j) { of[j][0] = *lp<v4u>(XIMG + j * 1024 + x.lane * 16); of[j][1] = *lp<v4u>(XIMG + (16 + j) * 1024 + x.lane * 16); }
    TIE4(1, acc[0][0], acc[0][1], acc[1][0], acc[1][1]);
    sfor<0, 32>([&](auto J) {
      constexpr int j = J.v >> 1, T = J.v & 1;
      if constexpr (j < 8) mf_av<false, AO(j, 0)>(acc[T][0], of[j % 3][T]); else mf_vv<false>(acc[T][0], WO[j - 8][0], of[j % 3][T]);
      if constexpr (j + 2 < 16) of[(j + 2) % 3][T] = *lp<v4u>(XIMG + (16 * T + j + 2) * 1024 + x.lane * 16);
      SB();
      if constexpr (j < 8) mf_av<false, AO(j, 1)>(acc[T][1], of[j % 3][T]); else mf_vv<false>(acc[T][1], WO[j - 8][1], of[j % 3][T]);
      SB();
    });
    TIE4(7, acc[0][0], acc[0][1], acc[1][0], acc[1][1]);
  }
  STAMP(8 + 12 * L);
#pragma unroll
  for (int T = 0; T < 2; ++T) {
    xk[T] = v4u{pk(acc[T][0][0], acc[T][0][1]), pk(acc[T][0][2], acc[T][0][3]), pk(acc[T][1][0], acc[T][1][1]), pk(acc[T][1][2], acc[T][1][3])};
    stc4(x.rS, x.lane * 16, xk[T], W_X + L * BIG_L + ((4 * x.c2 + 2 * x.u + T) * 16 + 4 * x.q + w) * 1024);
  }
  f32x4 dd[2];
  mf_vvn<true>(dd[0], wfr, xk[0]); mf_vvn<true>(dd[1], wfr, xk[1]);
  TIE2(7, dd[0], dd[1]);
#pragma unroll
  for (int T = 0; T < 2; ++T) {
    const f32x4 d = dd[T];
    float s2 = dot2(xk[T].x, xk[T].x, dot2(xk[T].y, xk[T].y, 0.f)) + dot2(xk[T].z, xk[T].z, dot2(xk[T].w, xk[T].w, 0.f));
    s2 = x32sum(x16sum(s2));
    if (x.g == 0) *lp<f32x4>(M_RED + (w * 32 + 16 * T + x.t) * 16) = f32x4{d[0], d[1], s2, 0.f};
  }
  BAR();
  if (w == 0) {
    const int tk = x.lane & 31, hf = x.lane >> 5;
    const unsigned pb = W_PST + L * PST_L + (x.c2 * 2 + x.u) * 2048;
    {
      const f32x4 a = *lp<f32x4>(M_RED + (hf * 64 + tk) * 16) + *lp<f32x4>(M_RED + (hf * 64 + 32 + tk) * 16);
      const f32x4 s = {x32sum(a[0]), x32sum(a[1]), x32sum(a[2]), 0.f};
      stc4(x.rS, hf ? OOB : x.q * 512 + tk * 16, __builtin_bit_cast(v4u, s), pb);
    }
    STAMP(57 + 3 * L);
    DRAIN();
    STAMP(58 + 3 * L);
    v4u a, b;
    for (int sp = 0; sp < SPIN; ++sp) {
      MEMF();
      a = ldc4(x.rS, hf * 1024 + tk * 16, pb); b = ldc4(x.rS, hf * 1024 + 512 + tk * 16, pb);
      if (__all(a.w != POISON && b.w != POISON)) break;
    }
    STAMP(59 + 3 * L);
    const float s1 = x32sum(f_(a.x) + f_(b.x)), sw = x32sum(f_(a.y) + f_(b.y)), s2 = x32sum(f_(a.z) + f_(b.z));
    const float m = s1 * (1.f / 512), var = fmaxf(s2 * (1.f / 512) - m * m, 0.f), rs = rsq(var + EPS);
    if (x.q == 0) {
      const float r = rs * (sw - m * wsum);
      unsigned k = u_(r); k = (k & 0x80000000u) ? ~k : (k | 0x80000000u);
      const int tok = x.c2 * 64 + x.u * 32 + tk;
      k = (k & ~0x7FFu) | (unsigned)(2047 - tok);
      const unsigned m1 = x16maxu(rowmaxu(k)), m2 = x16maxu(rowmaxu(k == m1 ? 0u : k));
      stc2(x.rS, x.lane == 0 ? (x.c2 * 2 + x.u) * 128 : OOB, v2u{m1, m2}, W_KEY + L * KEY_L);
      stc2(x.rS, hf ? OOB : tok * 8, v2u{u_(m), u_(var)}, W_ST + L * ST_L);
    }
    *lp<f32x2>(M_PS + tk * 8) = f32x2{m, rs};
  }
}
DEV void load_o(const Ctx& x, int L, v4u (&ob)[8]) {
  const unsigned so = W_O + L * BIG_L + ((x.c2 * 2 + x.u) * 32 + 8 * x.w) * 1024;
  for (int sp = 0; sp < SPIN; ++sp) {
    MEMF();
#pragma unroll
    for (int i = 0; i < 8; ++i) ob[i] = ldc4(x.rS, x.lane * 16 + i * 1024, so);
    const unsigned m = max(max(max(ob[0].x, ob[1].x), max(ob[2].x, ob[3].x)), max(max(ob[4].x, ob[5].x), max(ob[6].x, ob[7].x)));
    if (__all(m != POISON)) break;
  }
}
DEV void unpack8(v4u v, f32x4& a, f32x4& b) { a = f32x4{blo(v.x), bhi(v.x), blo(v.y), bhi(v.y)}; b = f32x4{blo(v.z), bhi(v.z), blo(v.w), bhi(v.w)}; }

__global__ void __launch_bounds__(256, 1)
fused(const u16* __restrict__ x0, const u16* __restrict__ wqkv, const u16* __restrict__ wo, const u16* __restrict__ wr, const u16* __restrict__ wg,
      const u16* __restrict__ wu, const u16* __restrict__ wd, u16* __restrict__ out, char* __restrict__ ws) {
  Ctx x;
  const int b = blockIdx.x, tid = threadIdx.x, lane = tid & 63, w = __builtin_amdgcn_readfirstlane(tid >> 6), t = lane & 15, g = lane >> 4;
  const int xc = b & 7, jb = b >> 3;
  x.lane = lane; x.w = w; x.t = t; x.g = g;
  x.c = xc * 4 + (jb >> 3); x.hg = jb & 7; x.c2 = x.c; x.u = x.hg >> 2; x.q = x.hg & 3;
  { const int f = (jb / 6) * 8 + xc; x.ff = (jb % 6 == 1 && f < NFF) ? f : -1; }
  const int c = x.c, hg = x.hg;
  x.rS = mk_rs(ws, W_END);
  const v4u sS = mk_srd(ws, W_END), sX = mk_srd(x0, 2048 * 1024), sW = mk_srd(wqkv, 512 * 3072), sO = mk_srd(wo, 512 * 1024);
  CLAIM_AGPRS();
  STAMP(0);

  v2u ep;
  asm volatile("buffer_load_dwordx2 %0, off, %1, 0 sc1" : "=v"(ep) : "s"(sS) : "memory");
  sfor<0, 20>([&](auto I) {
    constexpr int i = I.v;
    const int R = 20 * w + i, tok = (c == 0 && R < 16) ? R : 64 * c - 16 + R;
    dma<i * 1024, 0>(sX, (lane ^ (R & 15)) * 16, XIMG + 20 * w * 1024, tok * 1024);
  });
  const unsigned wvo = (8 * w + (lane >> 3)) * 3072 + hg * 128 + (((((lane & 7) >> 1) ^ (((lane >> 4) & 1) + 2 * (w & 1))) * 2) + (lane & 1)) * 16;
  const unsigned ringw = RING + w * 1024;
  auto issue_w = [&](auto S) {
    constexpr int s = S.v;
    sfor<0, 3>([&](auto P) { dma<(s % 6) * 12288 + P.v * 4096, s * 98304 + P.v * 1024>(sW, wvo, ringw, 0); });
  };
  const unsigned ovo = g * 1024 + x.q * 256 + (t ^ (4 * g)) * 16;
  auto issue_o_ring = [&](auto S) {
    constexpr int sl = S.v;
    sfor<0, 3>([&](auto P) { dma<(12 * sl + P.v) * 1024, (12 * sl + P.v) * 4096>(sO, ovo, RING + 3 * w * 1024, 3 * w * 4096); });
  };
  const v4u pz = {POISON, POISON, POISON, POISON};
  sfor<0, 5>([&](auto I) {
    constexpr int i = I.v;
    waitv<16 + i>();
    STAMP(50 + i);
    if constexpr (i < 2) {
      const unsigned ob = W_O + i * BIG_L + (c * 64 + 4 * hg + w) * 1024;
      stc4(x.rS, lane * 16, pz, ob); stc4(x.rS, lane * 16, pz, ob + 32768);
    } else if constexpr (i < 4) {
      const unsigned xb = W_X + (i - 2) * BIG_L + ((4 * x.c2 + 2 * x.u) * 16 + 4 * x.q + w) * 1024;
      stc4(x.rS, lane * 16, pz, xb); stc4(x.rS, lane * 16, pz, xb + 16384);
    } else {
      const int gi = x.c2 * 2 + x.u;
      unsigned so = OOB;
      if (w == 1) so = W_PST + (lane >> 5) * PST_L + gi * 2048 + x.q * 512 + (lane & 31) * 16;
      if (w == 2 && x.q == 0) so = lane < 32 ? W_ST + g * ST_L + gi * 256 + t * 16 : (lane < 34 ? W_KEY + (lane - 32) * KEY_L + gi * 128 : OOB);
      stc4(x.rS, so, pz);
      stc4(x.rS, x.ff >= 0 ? W_Y + (w >> 1) * Y_L + ((w & 1) * 44 + x.ff) * 1024 + lane * 16 : OOB, pz);
    }
    issue_w(I);
    const int R = 20 * w + 4 * i + g;
    const unsigned ra = XIMG + R * 1024 + t * 16;
    float s1a = 0.f, s1b = 0.f, s2a = 0.f, s2b = 0.f;
#pragma unroll
    for (int k = 0; k < 4; ++k) {
      const v4u v = *lp<v4u>(ra + k * 256);
      s1a = dot2(v.x, ONE2, s1a); s2a = dot2(v.x, v.x, s2a); s1b = dot2(v.y, ONE2, s1b); s2b = dot2(v.y, v.y, s2b);
      s1a = dot2(v.z, ONE2, s1a); s2a = dot2(v.z, v.z, s2a); s1b = dot2(v.w, ONE2, s1b); s2b = dot2(v.w, v.w, s2b);
    }
    const float m = rowsum(s1a + s1b) * (1.f / 512), var = fmaxf(rowsum(s2a + s2b) * (1.f / 512) - m * m, 0.f);
    *lp<float>(M_STM + R * 4) = m; *lp<float>(M_STA + R * 4) = rsq(var + EPS);
    if (i == 0 && w == 0) *lp<v4u>(XIMG + lane * 16) = v4u{ONE2, ONE2, ONE2, ONE2};
    SB();
  });
  asm volatile("" : "+v"(ep));
  const unsigned e0 = rfl(ep.x) + 1u, e1 = rfl(ep.y);
  STAMP(1);

  Acc a;
  {
    unsigned xb[4];
#pragma unroll
    for (int k = 0; k < 4; ++k) xb[k] = XIMG + t * 1024 + (((4 * k) ^ (t & 12)) | (g ^ (t & 3))) * 16;
    const int qq = t >> 2;
    const unsigned wb = RING + (8 * g + qq) * 128 + (w ^ ((qq >> 1) + 2 * (g & 1))) * 32 + 8 * (lane & 3);
    const unsigned wb2 = wb + 36864;
    v4u xf[2][5];
    sfor<0, 17>([&](auto S) {
      constexpr int s = S.v;
      if constexpr (s < 16) {
        waitv<(s < 4 ? 20 - 2 * s : 12)>();
        BAR();
        STAMP(32 + s);
      }
      auto mm = [&](auto N) {
        constexpr int n = N.v;
        if constexpr (s > 0) {
          constexpr int i = n / 3, p = n % 3, z = (s - 1) & 1;
          if constexpr (p == 0) mf_av<s == 1, AW(s - 1, 1)>(a.K[i], xf[z][i]);
          if constexpr (p == 1) mf_va<s == 1, AW(s - 1, 2)>(a.V[i], xf[z][i]);
          if constexpr (p == 2) mf_av<s == 1, AW(s - 1, 0)>(a.Q[i], xf[z][i]);
        }
        SB();
      };
      sfor<0, 15>([&](auto N) {
        constexpr int n = N.v;
        mm(N);
        if constexpr (s < 16) {
          if constexpr (n < 3) {
            if constexpr (s + 5 < 16) dma<((s + 5) % 6) * 12288 + n * 4096, (s + 5) * 98304 + n * 1024>(sW, wvo, ringw, 0);
            else dma<(12 * ((s + 5) % 6) + n) * 1024, (12 * ((s + 5) % 6) + n) * 4096>(sO, ovo, RING + 3 * w * 1024, 3 * w * 4096);
          } else if constexpr (n < 6) {
            constexpr int o = (s % 3) * 12288 + (n - 3) * 4096;
            trd_a<AW(s, n - 3), o, o + 512>((s % 6) < 3 ? wb : wb2);
          } else if constexpr (n < 11) {
            xf[s & 1][n - 6] = *lp<v4u>(xb[s & 3] + (n - 6) * 16384 + (s >> 2) * 256);
          } else if constexpr (n == 11 && s == 4) {
            stc2(x.rS, lane == 0 ? W_FLAGS + (b * 4 + w) * 8 : OOB, v2u{e0, e1});
          }
          SB();
        }
      });
      if constexpr (s == 15) {
        BAR();
      }
    });
  }
  TIE3(7, a.Q[4], a.K[4], a.V[4]);
#pragma unroll
  for (int i = 0; i < 4; ++i) asm volatile("" : "+v"(a.Q[i]), "+v"(a.K[i]), "+v"(a.V[i]));
  STAMP(2);
  const rsrc_t rX = mk_rs(x0, 2048 * 1024), rR = mk_rs(wr, 1024);
  const int n0 = 128 * x.q + 32 * w;
  v4u xk[2];
#pragma unroll
  for (int T = 0; T < 2; ++T) xk[T] = ldp4(rX, (16 * T + t) * 1024 + g * 16, (64 * x.c2 + 32 * x.u) * 1024 + n0 * 2);
  const v4u wr8 = ldp4(rR, g * 16, n0 * 2), wrall = ldp4(rR, lane * 16);
  issue_o_ring(IC<3>{});
  sfor<0, 14>([&](auto K) { dma<K.v * 1024, K.v * 4096>(sO, ovo, XIMG + 24576 + 14 * w * 1024, (72 + 14 * w) * 4096); });
  v4u fl0 = ldc4(x.rS, lane * 16, W_FLAGS + w * 2048), fl1 = ldc4(x.rS, lane * 16 + 1024, W_FLAGS + w * 2048);
  SB();
  STAMP(56);
  if (t == 0) { *lp<f32x4>(M_CS + w * 192 + g * 16) = a.Q[0]; *lp<f32x4>(M_CS + w * 192 + 64 + g * 16) = a.K[0]; }
  if (g == 0) *lp<float>(M_CS + w * 192 + 128 + t * 4) = a.V[0][0];
  attention(x, a, 0);
  STAMP(3);
  v4u WO[8][2];
  const unsigned obs = (4 * g + (t >> 2)) * 256 + ((4 * w + (lane & 3)) ^ (t & 12)) * 16;
  waitv<18>();
  BAR();
  sfor<0, 16>([&](auto J) { constexpr int j = J.v >> 1, ct = J.v & 1; trd_a<AO(j, ct), j * 8192 + 8 * ct, j * 8192 + 4096 + 8 * ct>(RING + obs); });
#pragma unroll
  for (int ct = 0; ct < 2; ++ct) WO[0][ct] = trd2(RING + 65536 + obs + 8 * ct, RING + 65536 + obs + 4096 + 8 * ct);
  SB();
  DRAIN();
  for (int sp = 0; sp < SPIN; ++sp) {
    if (__all(fl0.x == e0 && fl0.y == e1 && fl0.z == e0 && fl0.w == e1 && fl1.x == e0 && fl1.y == e1 && fl1.z == e0 && fl1.w == e1)) break;
    MEMF();
    fl0 = ldc4(x.rS, lane * 16, W_FLAGS + w * 2048); fl1 = ldc4(x.rS, lane * 16 + 1024, W_FLAGS + w * 2048);
  }
  BAR();
  STAMP(4);
  if (b == 0 && w == 3) stc2(x.rS, lane == 0 ? W_EPOCH : OOB, v2u{e0, e1});
  v4u ob[8];
  {
    const unsigned so = W_O + ((x.c2 * 2 + x.u) * 32 + 8 * w) * 1024;
#pragma unroll
    for (int i = 0; i < 8; ++i) ob[i] = ldc4(x.rS, lane * 16 + i * 1024, so);
    SB();
#pragma unroll
    for (int j = 9; j < 16; ++j)
#pragma unroll
      for (int ct = 0; ct < 2; ++ct) WO[j - 8][ct] = trd2(XIMG + 24576 + obs + (j - 9) * 8192 + 8 * ct, XIMG + 24576 + obs + (j - 9) * 8192 + 4096 + 8 * ct);
    SB();
    const unsigned m = max(max(max(ob[0].x, ob[1].x), max(ob[2].x, ob[3].x)), max(max(ob[4].x, ob[5].x), max(ob[6].x, ob[7].x)));
    if (!__all(m != POISON)) load_o(x, 0, ob);
  }
  STAMP(5);
  const v4u wfr = t == 0 ? v4u{ONE2, ONE2, ONE2, ONE2} : (t == 1 ? wr8 : v4u{0u, 0u, 0u, 0u});
  const float wsum = x32sum(x16sum(rowsum(dot2(wrall.x, ONE2, dot2(wrall.y, ONE2, 0.f)) + dot2(wrall.z, ONE2, dot2(wrall.w, ONE2, 0.f)))));
  f32x4 acc[2][2];
#pragma unroll
  for (int T = 0; T < 2; ++T) unpack8(xk[T], acc[T][0], acc[T][1]);
  BAR();
  if (x.ff >= 0) {
    const v4u sG = mk_srd(wg, 512 * 2752), sU = mk_srd(wu, 512 * 2752);
    const unsigned gvo = (lane >> 2) * 2752 + x.ff * 64 + (((((lane >> 1) & 1) ^ (lane >> 5)) * 2) + (lane & 1)) * 16;
    sfor<0, 8>([&](auto K) {
      dma<K.v * 1024, K.v * 16 * 2752>(sG, gvo, RING + w * 8192, w * 128 * 2752);
      dma<32768 + K.v * 1024, K.v * 16 * 2752>(sU, gvo, RING + w * 8192, w * 128 * 2752);
    });
  }
  outproj(x, WO, ob, acc, wfr, wsum, 0, xk);
  STAMP(9);
  const rsrc_t rD = mk_rs(wd, 1376 * 1024);
  if (w == 1) {
    DRAIN();
    const int gi = max(2 * c - 1 + (t >> 2), 0);
    const unsigned o = t < 12 ? gi * 2048 + (t & 3) * 512 + 12 : OOB;
    for (int sp = 0; sp < SPIN; ++sp) {
      MEMF();
      if (__all(ldc1(x.rS, o, W_PST) != POISON)) break;
    }
  }
  BAR();
  STAMP(10);

  unsigned k1 = 0, k2 = 0;
  auto issue_x1 = [&]() {
#pragma unroll
    for (int k = 0; k < 5; ++k) {
      const int n = 20 * w + 4 * k;
      dma4_sc1(sS, lane * 16, XIMG + n * 1024, W_X + ((4 * c - 1) * 16 + n + ((c == 0 && n < 16) ? 16 : 0)) * 1024);
    }
  };
  issue_x1();
  if (x.ff >= 0) {
    v4u raw[8]; Ffn F;
    ffn_load_down(x, rD, raw);
    DRAIN();
    ffn_prep(x, raw, F);
    top2(x, 0, k1, k2, false);
    STAMP(11);
    ffn_run(x, F, 0, k1, k2);
    STAMP(12);
  }
#pragma unroll 1
  for (int tries = 0; tries < 64; ++tries) {
    DRAIN();
    BAR();
    STAMP(28);
    v4u xf[2][5];
#pragma unroll
    for (int i = 0; i < 5; ++i) xf[0][i] = *lp<v4u>(XIMG + i * 16384 + lane * 16);
    sfor<0, 16>([&](auto S) {
      constexpr int s = S.v;
      sfor<0, 5>([&](auto I) {
        constexpr int i = I.v;
        mf_av<s == 0, AW(s, 1)>(a.K[i], xf[s & 1][i]);
        if constexpr (s < 15) xf[(s + 1) & 1][i] = *lp<v4u>(XIMG + (i * 16 + s + 1) * 1024 + lane * 16);
        SB();
        mf_va<s == 0, AW(s, 2)>(a.V[i], xf[s & 1][i]);
        if constexpr (i > 0) mf_av<s == 0, AW(s, 0)>(a.Q[i], xf[s & 1][i]);
        SB();
      });
    });
    TIE3(7, a.Q[4], a.K[4], a.V[4]);
#pragma unroll
    for (int i = 1; i < 4; ++i) asm volatile("" : "+v"(a.Q[i]), "+v"(a.K[i]), "+v"(a.V[i]));
    TIE0_2(a.K[0], a.V[0]);
    const float chk = (a.K[0][0] + a.K[1][0]) + (a.K[2][0] + a.K[3][0]) + a.K[4][0];
    if (!__any(isnan_(chk))) break;
    BAR();
    issue_x1();
  }
  STAMP(13);
  if (w == 1) {
    v2u sa, sb;
    const int ta = 64 * c - 16 + lane;
    for (int sp = 0; sp < SPIN; ++sp) {
      MEMF();
      sa = ldc2(x.rS, max(ta, 0) * 8, W_ST); sb = ldc2(x.rS, (64 * c + 48 + t) * 8, W_ST);
      if (__all(sa.x != POISON && sb.x != POISON)) break;
    }
    const float ra = 1.f + rsq(f_(sa.y) + EPS), rb = 1.f + rsq(f_(sb.y) + EPS);
    *lp<float>(M_STM + lane * 4) = f_(sa.x); *lp<float>(M_STA + lane * 4) = ra * rsq(f_(sa.y) * ra * ra + EPS);
    *lp<float>(M_STM + (64 + t) * 4) = f_(sb.x); *lp<float>(M_STA + (64 + t) * 4) = rb * rsq(f_(sb.y) * rb * rb + EPS);
  }
  if (x.ff < 0) top2(x, 0, k1, k2, false); else BAR();
  STAMP(14);
#pragma unroll 1
  for (int sl = 0; sl < 2; ++sl) {
    const int tau = key_tok(sl ? k2 : k1), R = tau - (64 * c - 16);
    if (R < 2 || R >= 80 || (c == 0 && R < 16)) continue;
    const v4u sel = sel_frag(t);
    const unsigned o = (sl * 44 + g) * 1024 + w * 256 + t * 16;
    const v2u xr = *lp<v2u>(XIMG + ((R >> 4) * 16 + 4 * w + (t >> 2)) * 1024 + ((t & 3) * 16 + (R & 15)) * 16 + (g & 1) * 8);
    const v2u st = ldc2(x.rS, tau * 8, W_ST);
    f32x4 d;
    for (int sp = 0; sp < SPIN; ++sp) {
      MEMF();
      v4u y[11];
#pragma unroll
      for (int i = 0; i < 11; ++i) y[i] = ldc4(x.rS, (i == 10 && g == 3) ? OOB : o + i * 4096, W_Y);
      unsigned m = 0;
#pragma unroll
      for (int i = 0; i < 11; ++i) { if (i == 0) mf_vvn<true>(d, sel, y[i]); else mf_vv<false>(d, sel, y[i]); m = max(m, y[i].x); }
      TIE1(7, d);
      if (__all(m != POISON)) break;
    }
    STAMP(15);
    const float m1 = f_(st.x), rs1 = rsq(f_(st.y) + EPS);
    const f32x4 xv = {blo(xr.x), bhi(xr.x), blo(xr.y), bhi(xr.y)};
    const f32x4 x2 = xv * (1.f + rs1) - m1 * rs1 + d;
    const v2u xp = {pk(x2[0], x2[1]), pk(x2[2], x2[3])};
    float s1 = dot2(xp.x, ONE2, dot2(xp.y, ONE2, 0.f)), s2 = dot2(xp.x, xp.x, dot2(xp.y, xp.y, 0.f));
    s1 = x16sum(rowsum(s1)); s2 = x16sum(rowsum(s2));
    if (g < 2) { *lp<v2u>(M_FIX + w * 256 + t * 16 + g * 8) = xp; *lp<f32x2>(M_FST + w * 8) = f32x2{s1, s2}; }
    BAR();
    f32x4 fq, fk, fv;
    {
      v4u xf[4];
      const unsigned fb = M_FIX + g * 16;
#pragma unroll
      for (int s = 0; s < 3; ++s) xf[s] = *lp<v4u>(fb + s * 64);
      sfor<0, 16>([&](auto S) {
        constexpr int s = S.v;
        mf_av<s == 0, AW(s, 0)>(fq, xf[s & 3]);
        if constexpr (s + 3 < 16) xf[(s + 3) & 3] = *lp<v4u>(fb + (s + 3) * 64);
        SB();
        mf_av<s == 0, AW(s, 1)>(fk, xf[s & 3]); mf_va<s == 0, AW(s, 2)>(fv, xf[s & 3]);
        SB();
      });
      TIE3(7, fq, fk, fv);
    }
    const f32x4 sa = *lp<f32x4>(M_FST), sb = *lp<f32x4>(M_FST + 16);
    const float m2 = ((sa[0] + sa[2]) + (sb[0] + sb[2])) * (1.f / 512);
    const float v2 = fmaxf(((sa[1] + sa[3]) + (sb[1] + sb[3])) * (1.f / 512) - m2 * m2, 0.f);
    const bool mt = t == (R & 15);
#pragma unroll
    for (int i = 0; i < 5; ++i)
      if ((R >> 4) == i) {
#pragma unroll
        for (int r = 0; r < 4; ++r) {
          if (i > 0) a.Q[i][r] = mt ? fq[r] : a.Q[i][r];
          a.K[i][r] = mt ? fk[r] : a.K[i][r];
          a.V[i][r] = (4 * g + r == (R & 15)) ? fv[r] : a.V[i][r];
        }
      }
    *lp<float>(M_STM + R * 4) = m2; *lp<float>(M_STA + R * 4) = rsq(v2 + EPS);
    BAR();
  }
  STAMP(16);
  attention(x, a, 1);
  STAMP(17);
#pragma unroll
  for (int T = 0; T < 2; ++T) {
    const f32x2 ps = *lp<f32x2>(M_PS + (16 * T + t) * 8);
    unpack8(xk[T], acc[T][0], acc[T][1]);
    acc[T][0] = acc[T][0] * (1.f + ps[1]) - ps[0] * ps[1]; acc[T][1] = acc[T][1] * (1.f + ps[1]) - ps[0] * ps[1];
  }
#pragma unroll 1
  for (int sl = 0; sl < 2; ++sl) {
    const int R = key_tok(sl ? k2 : k1) - (64 * x.c2 + 32 * x.u);
    if (R < 0 || R >= 32) continue;
    y_slice(x, 0, sl);
    const f32x4 ya = *lp<f32x4>(M_YS + w * 128 + g * 32), yb = *lp<f32x4>(M_YS + w * 128 + g * 32 + 16);
#pragma unroll
    for (int T = 0; T < 2; ++T) { const float f = (16 * T + t == R) ? 1.f : 0.f; acc[T][0] += ya * f; acc[T][1] += yb * f; }
  }
  DRAIN();
  STAMP(18);
  load_o(x, 1, ob);
  STAMP(19);
  BAR();
  outproj(x, WO, ob, acc, wfr, wsum, 1, xk);
  STAMP(21);
  BAR();
  const rsrc_t rOut = mk_rs(out, 2048 * 1024);
  const unsigned oso = (64 * x.c2 + 32 * x.u) * 1024 + n0 * 2;
#pragma unroll
  for (int T = 0; T < 2; ++T) {
    const f32x2 ps = *lp<f32x2>(M_PS + (16 * T + t) * 8);
    unpack8(xk[T], acc[T][0], acc[T][1]);
    acc[T][0] = acc[T][0] * (1.f + ps[1]) - ps[0] * ps[1]; acc[T][1] = acc[T][1] * (1.f + ps[1]) - ps[0] * ps[1];
    __builtin_amdgcn_raw_buffer_store_b128(v4u{pk(acc[T][0][0], acc[T][0][1]), pk(acc[T][0][2], acc[T][0][3]), pk(acc[T][1][0], acc[T][1][1]), pk(acc[T][1][2], acc[T][1][3])},
                                           rOut, (16 * T + t) * 1024 + g * 16, oso, 0);
  }
  STAMP(22);
  if (x.ff >= 0) {
    v4u raw[8]; Ffn F;
    ffn_load_down(x, rD, raw);
    ffn_prep(x, raw, F);
    top2(x, 1, k1, k2, false);
    STAMP(23);
    ffn_run(x, F, 1, k1, k2);
    STAMP(24);
  } else top2(x, 1, k1, k2, true);
  STAMP(25);
#pragma unroll 1
  for (int sl = 0; sl < 2; ++sl) {
    const int R = key_tok(sl ? k2 : k1) - (64 * x.c2 + 32 * x.u);
    if (R < 0 || R >= 32) continue;
    y_slice(x, 1, sl);
    const f32x4 ya = *lp<f32x4>(M_YS + w * 128 + g * 32), yb = *lp<f32x4>(M_YS + w * 128 + g * 32 + 16);
    const f32x4 a0 = (R < 16 ? acc[0][0] : acc[1][0]) + ya, a1 = (R < 16 ? acc[0][1] : acc[1][1]) + yb;
    __builtin_amdgcn_raw_buffer_store_b128(v4u{pk(a0[0], a0[1]), pk(a0[2], a0[3]), pk(a1[0], a1[1]), pk(a1[2], a1[3])},
                                           rOut, t == (R & 15) ? R * 1024 + g * 16 : OOB, oso, 0);
  }
  STAMP(26);
}
}

extern "C" void kernel_launch(void* const* d_in, const int* in_sizes, int n_in, void* d_out, int out_size, void* d_ws, size_t ws_size, hipStream_t stream) {
  fused<<<256, 256, 0, stream>>>((const u16*)d_in[0], (const u16*)d_in[1], (const u16*)d_in[2], (const u16*)d_in[4], (const u16*)d_in[5],
                                 (const u16*)d_in[6], (const u16*)d_in[7], (u16*)d_out, (char*)d_ws);
}
```

```cpp
#define PROBE 50
#include <hip/hip_runtime.h>

namespace {
typedef unsigned short u16;
typedef unsigned long long u64;
typedef unsigned v4u __attribute__((ext_vector_type(4)));
typedef unsigned v2u __attribute__((ext_vector_type(2)));
typedef float f32x4 __attribute__((ext_vector_type(4)));
typedef float f32x2 __attribute__((ext_vector_type(2)));
typedef __bf16 bf16x8 __attribute__((ext_vector_type(8)));
typedef __bf16 bf16x2 __attribute__((ext_vector_type(2)));
typedef short s16x4 __attribute__((ext_vector_type(4)));
typedef __amdgpu_buffer_rsrc_t rsrc_t;
#define LAS __attribute__((address_space(3)))
#define DEV __device__ __forceinline__

#define MAPPING 0

constexpr int W_EPOCH = 0, W_FLAGS = 4096, W_KEY = 16384, W_ST = 32768, W_PST = 65536, W_Y = 393216;
constexpr int KEY_L = 8192, ST_L = 16384, PST_L = 131072, Y_L = 98304;
constexpr int W_O = 1 << 20, W_X = 5 << 20, BIG_L = 2 << 20, W_END = 9 << 20;
constexpr int W_DBG = 16 << 20;
constexpr unsigned OOB = 0x7ff00000u;
constexpr unsigned POISON = 0xFFFFFFFFu;
constexpr int NFF = 43;
constexpr int SPIN = 1 << 15;
constexpr float EPS = 1e-5f;
constexpr int XIMG = 0, RING = 81920, MISC = 155648, LDS_BYTES = 163840;
constexpr int M_STM = MISC, M_STA = MISC + 320, M_RED = MISC + 1024, M_PS = MISC + 3072, M_TOP = MISC + 3328, M_HF = MISC + 3584,
              M_FIX = MISC + 4096, M_FST = MISC + 5120, M_YS = MISC + 5376, M_DBG = MISC + 6144, M_CS = MISC + 7168;

__shared__ __attribute__((aligned(16))) char smem[LDS_BYTES];
template <class T> DEV LAS T* lp(unsigned off) { return (LAS T*)((LAS char*)smem + off); }

#define MEMF() asm volatile("" ::: "memory")
#define WAITV(n) asm volatile("s_waitcnt vmcnt(" #n ")" ::: "memory")
#define WAITL() asm volatile("s_waitcnt lgkmcnt(0)" ::: "memory")
#define DRAIN() WAITV(0)
#define BAR() do { WAITL(); __builtin_amdgcn_s_barrier(); MEMF(); } while (0)
#define SB() __builtin_amdgcn_sched_barrier(0)
template <int N> DEV void waitv() { asm volatile("s_waitcnt vmcnt(%0)" ::"n"(N) : "memory"); }

#define STAMP(i) do { if ((i) == 0) t_a = __builtin_amdgcn_s_memrealtime(); if ((i) == PROBE) t_b = __builtin_amdgcn_s_memrealtime(); } while (0)

#define STAMPI(i)
DEV v4u mk_srd(const void* p, unsigned bytes) { const u64 a = (u64)p; return v4u{(unsigned)a, (unsigned)(a >> 32) & 0xffffu, bytes, 0x00020000u}; }
DEV rsrc_t mk_rs(const void* p, unsigned bytes) { return __builtin_amdgcn_make_buffer_rsrc((void*)p, 0, bytes, 0x00020000); }
template <int LI, int SI> DEV void dma(v4u srd, unsigned vo, unsigned lb, unsigned sb) {
  unsigned t;
  asm volatile("s_add_i32 m0, %3, %5\n\ts_add_i32 %0, %4, %6\n\tbuffer_load_dwordx4 %1, %2, %0 offen lds"
               : "=&s"(t) : "v"(vo), "s"(srd), "s"(lb), "s"(sb), "n"(LI), "n"(SI) : "memory", "scc");
}
DEV void dma4_sc1(v4u srd, unsigned vo, unsigned lb, unsigned sb) {
  asm volatile("s_mov_b32 m0, %2\n\ts_nop 0\n\tbuffer_load_dwordx4 %0, %1, %3 offen sc1 lds\n\tbuffer_load_dwordx4 %0, %1, %3 offen offset:1024 sc1 lds\n\t"
               "buffer_load_dwordx4 %0, %1, %3 offen offset:2048 sc1 lds\n\tbuffer_load_dwordx4 %0, %1, %3 offen offset:3072 sc1 lds"
               ::"v"(vo), "s"(srd), "s"(lb), "s"(sb) : "memory");
}
DEV v4u ldc4(rsrc_t r, unsigned vo, unsigned so = 0) { return __builtin_amdgcn_raw_buffer_load_b128(r, vo, so, 16); }
DEV v2u ldc2(rsrc_t r, unsigned vo, unsigned so = 0) { return __builtin_amdgcn_raw_buffer_load_b64(r, vo, so, 16); }
DEV unsigned ldc1(rsrc_t r, unsigned vo, unsigned so = 0) { return __builtin_amdgcn_raw_buffer_load_b32(r, vo, so, 16); }
DEV void stc4(rsrc_t r, unsigned vo, v4u v, unsigned so = 0) { __builtin_amdgcn_raw_buffer_store_b128(v, r, vo, so, 16); }
DEV void stc2(rsrc_t r, unsigned vo, v2u v, unsigned so = 0) { __builtin_amdgcn_raw_buffer_store_b64(v, r, vo, so, 16); }
DEV v4u ldp4(rsrc_t r, unsigned vo, unsigned so = 0) { return __builtin_amdgcn_raw_buffer_load_b128(r, vo, so, 0); }

#define MF_(op, pre, d, a, b, ca, cb) asm volatile(pre op " %0, %1, %2, %0" : "+v"(d) : ca(a), cb(b))
#define MFZ_(op, pre, d, a, b, ca, cb) asm volatile(pre op " %0, %1, %2, 0" : "=&v"(d) : ca(a), cb(b))
#define MFC_(op, pre, d, a, b, c) asm volatile(pre op " %0, %1, %2, %3" : "=&v"(d) : "v"(a), "v"(b), "v"(c))
#define M32 "v_mfma_f32_16x16x32_bf16"
#define M16 "v_mfma_f32_16x16x16_bf16"
#define NOP2 "s_nop 1\n\t"
constexpr int AW(int s, int p) { return (s * 3 + p) * 4; }
constexpr int AO(int j, int ct) { return 192 + (j * 2 + ct) * 4; }
template <bool Z, int A> DEV void mf_av(f32x4& d, const v4u& b) {
  if constexpr (Z) asm volatile(M32 " %0, a[%c2:%c3], %1, 0" : "=&v"(d) : "v"(b), "n"(A), "n"(A + 3));
  else asm volatile(M32 " %0, a[%c2:%c3], %1, %0" : "+v"(d) : "v"(b), "n"(A), "n"(A + 3));
}
template <bool Z, int A> DEV void mf_va(f32x4& d, const v4u& b) {
  if constexpr (Z) asm volatile(M32 " %0, %1, a[%c2:%c3], 0" : "=&v"(d) : "v"(b), "n"(A), "n"(A + 3));
  else asm volatile(M32 " %0, %1, a[%c2:%c3], %0" : "+v"(d) : "v"(b), "n"(A), "n"(A + 3));
}
template <int A, int O0, int O1> DEV void trd_a(unsigned base) {
  static_assert(O0 < 65536 && O1 < 65536, "DS offset");
  asm volatile("ds_read_b64_tr_b16 a[%c1:%c2], %0 offset:%c5\n\tds_read_b64_tr_b16 a[%c3:%c4], %0 offset:%c6"
               ::"v"(base), "n"(A), "n"(A + 1), "n"(A + 2), "n"(A + 3), "n"(O0), "n"(O1) : "memory");
}
#define A8(n) "a" #n "0", "a" #n "1", "a" #n "2", "a" #n "3", "a" #n "4", "a" #n "5", "a" #n "6", "a" #n "7", "a" #n "8", "a" #n "9"
#define CLAIM_AGPRS() asm volatile("" ::: A8(), A8(1), A8(2), A8(3), A8(4), A8(5), A8(6), A8(7), A8(8), A8(9), A8(10), A8(11), A8(12), A8(13), A8(14), A8(15), A8(16), \
    A8(17), A8(18), A8(19), A8(20), A8(21), A8(22), A8(23), A8(24), "a250", "a251", "a252", "a253", "a254", "a255")
template <bool Z> DEV void mf_vv(f32x4& d, const v4u& a, const v4u& b) { if constexpr (Z) MFZ_(M32, "", d, a, b, "v", "v"); else MF_(M32, "", d, a, b, "v", "v"); }
template <bool Z> DEV void mf_vvn(f32x4& d, const v4u& a, const v4u& b) { if constexpr (Z) MFZ_(M32, NOP2, d, a, b, "v", "v"); else MF_(M32, NOP2, d, a, b, "v", "v"); }
#define TIE1(n, a) asm volatile("s_nop " #n : "+v"(a))
#define TIE2(n, a, b) asm volatile("s_nop " #n : "+v"(a), "+v"(b))
#define TIE3(n, a, b, c) asm volatile("s_nop " #n : "+v"(a), "+v"(b), "+v"(c))
#define TIE4(n, a, b, c, d) asm volatile("s_nop " #n : "+v"(a), "+v"(b), "+v"(c), "+v"(d))
#define TIE0_2(a, b) asm volatile("" : "+v"(a), "+v"(b))
#define TIE0_4(a, b, c, d) asm volatile("" : "+v"(a), "+v"(b), "+v"(c), "+v"(d))
DEV v2u trd(unsigned off) { return __builtin_bit_cast(v2u, __builtin_amdgcn_ds_read_tr16_b64_v4i16(lp<s16x4>(off))); }
DEV v4u trd2(unsigned o0, unsigned o1) { const v2u a = trd(o0), b = trd(o1); return v4u{a.x, a.y, b.x, b.y}; }
DEV unsigned pk(float a, float b) { return __builtin_bit_cast(unsigned, __builtin_convertvector((f32x2{a, b}), bf16x2)); }
DEV float blo(unsigned v) { return __builtin_bit_cast(float, v << 16); }
DEV float bhi(unsigned v) { return __builtin_bit_cast(float, v & 0xffff0000u); }
DEV float dot2(unsigned a, unsigned b, float c) { return __builtin_amdgcn_fdot2_f32_bf16(__builtin_bit_cast(bf16x2, a), __builtin_bit_cast(bf16x2, b), c, false); }
DEV float f_(unsigned v) { return __builtin_bit_cast(float, v); }
DEV unsigned u_(float v) { return __builtin_bit_cast(unsigned, v); }
template <int C> DEV unsigned dppu(unsigned v) { return (unsigned)__builtin_amdgcn_update_dpp(0, (int)v, C, 0xF, 0xF, true); }
template <int C> DEV float dppf(float v) { return f_(dppu<C>(u_(v))); }
DEV float rowsum(float v) { v += dppf<0x128>(v); v += dppf<0x124>(v); v += dppf<0x122>(v); v += dppf<0x121>(v); return v; }
DEV unsigned rowmaxu(unsigned v) { v = max(v, dppu<0x128>(v)); v = max(v, dppu<0x124>(v)); v = max(v, dppu<0x122>(v)); v = max(v, dppu<0x121>(v)); return v; }
DEV float x16sum(float v) { auto r = __builtin_amdgcn_permlane16_swap(u_(v), u_(v), false, false); return f_(r[0]) + f_(r[1]); }
DEV float x32sum(float v) { auto r = __builtin_amdgcn_permlane32_swap(u_(v), u_(v), false, false); return f_(r[0]) + f_(r[1]); }
DEV float x16max(float v) { auto r = __builtin_amdgcn_permlane16_swap(u_(v), u_(v), false, false); return fmaxf(f_(r[0]), f_(r[1])); }
DEV float x32max(float v) { auto r = __builtin_amdgcn_permlane32_swap(u_(v), u_(v), false, false); return fmaxf(f_(r[0]), f_(r[1])); }
DEV unsigned x16maxu(unsigned v) { auto r = __builtin_amdgcn_permlane16_swap(v, v, false, false); return max((unsigned)r[0], (unsigned)r[1]); }
DEV unsigned x32maxu(unsigned v) { auto r = __builtin_amdgcn_permlane32_swap(v, v, false, false); return max((unsigned)r[0], (unsigned)r[1]); }
DEV unsigned rfl(unsigned v) { return (unsigned)__builtin_amdgcn_readfirstlane((int)v); }
DEV float rsq(float v) { return __builtin_amdgcn_rsqf(v); }
DEV float rcp(float v) { return __builtin_amdgcn_rcpf(v); }
DEV float ex2(float v) { return __builtin_amdgcn_exp2f(v); }
DEV bool isnan_(float v) { return v != v; }
constexpr unsigned ONE2 = 0x3F803F80u;
constexpr float LOG2E = 1.4426950408889634f;

template <int S> struct IC { static constexpr int v = S; };
template <int A, int B, class F> DEV void sfor(F&& f) { if constexpr (A < B) { f(IC<A>{}); sfor<A + 1, B>(f); } }

struct Ctx {
  int lane, w, t, g, c, hg, c2, u, q, ff;
  rsrc_t rS;
  DEV void fresh() { asm volatile("" : "+v"(lane)); t = lane & 15; g = lane >> 4; }
};

struct Acc { f32x4 Q[5], K[5], V[5]; };

DEV void attention(Ctx& x, const Acc& a, int L) {
  x.fresh();
  const int t = x.t, g = x.g;
  const f32x4 csq = *lp<f32x4>(M_CS + x.w * 192 + g * 16), csk = *lp<f32x4>(M_CS + x.w * 192 + 64 + g * 16);
  const float csv = *lp<float>(M_CS + x.w * 192 + 128 + t * 4);
  v2u Qp[5], Kp[5], Vp[5];
  float cq[5];
#pragma unroll
  for (int i = 0; i < 5; ++i) {
    const float m = *lp<float>(M_STM + (16 * i + t) * 4), s = *lp<float>(M_STA + (16 * i + t) * 4);
    const f32x4 m4 = *lp<f32x4>(M_STM + (16 * i + 4 * g) * 4), s4 = *lp<f32x4>(M_STA + (16 * i + 4 * g) * 4);
    const f32x4 k = (a.K[i] - m * csk) * s;
    Kp[i] = v2u{pk(k[0], k[1]), pk(k[2], k[3])};
    const f32x4 v = (a.V[i] - m4 * csv) * s4;
    Vp[i] = v2u{pk(v[0], v[1]), pk(v[2], v[3])};
    if (i > 0) { const f32x4 qq = a.Q[i] - m * csq; Qp[i] = v2u{pk(qq[0], qq[1]), pk(qq[2], qq[3])}; cq[i] = s * (0.25f * LOG2E); }
  }
  f32x4 bp, bc;
#pragma unroll
  for (int r = 0; r < 4; ++r) {
    const int kk = 4 * g + r; const bool par = ((t ^ r) & 1) == 0;
    bc[r] = (par && kk <= t) ? 0.f : -1e30f;
    bp[r] = (par && kk >= t + 2) ? 0.f : -1e30f;
  }
  const v4u ones = {ONE2, ONE2, ONE2, ONE2};
  f32x4 b0 = bp;
  if (x.c == 0) b0 = f32x4{-1e30f, -1e30f, -1e30f, -1e30f};
  f32x4 sp[5], sc[5], o[5], l[5];
#pragma unroll
  for (int i = 1; i < 5; ++i) {
    if (i == 1) MFC_(M16, NOP2, sp[i], Kp[i - 1], Qp[i], b0); else MFC_(M16, NOP2, sp[i], Kp[i - 1], Qp[i], bp);
    MFC_(M16, NOP2, sc[i], Kp[i], Qp[i], bc);
  }
  v2u Op[5];
#pragma unroll
  for (int i = 1; i < 5; ++i) {
    TIE0_2(sp[i], sc[i]);
    float mx = fmaxf(fmaxf(fmaxf(sp[i][0], sp[i][1]), fmaxf(sp[i][2], sp[i][3])), fmaxf(fmaxf(sc[i][0], sc[i][1]), fmaxf(sc[i][2], sc[i][3])));
    mx = x32max(x16max(mx));
    const float cm = -cq[i] * mx;
    const f32x4 ep = sp[i] * cq[i] + cm, ec = sc[i] * cq[i] + cm;
    const v4u P = {pk(ex2(ep[0]), ex2(ep[1])), pk(ex2(ep[2]), ex2(ep[3])), pk(ex2(ec[0]), ex2(ec[1])), pk(ex2(ec[2]), ex2(ec[3]))};
    const v4u VV = {Vp[i - 1].x, Vp[i - 1].y, Vp[i].x, Vp[i].y};
    mf_vvn<true>(l[i], ones, P);
    mf_vvn<true>(o[i], VV, P);
    if (i > 1) {
      TIE0_2(o[i - 1], l[i - 1]);
      const f32x4 on = o[i - 1] * rcp(l[i - 1][0]);
      Op[i - 1] = v2u{pk(on[0], on[1]), pk(on[2], on[3])};
    }
  }
  TIE2(7, o[4], l[4]);
  { const f32x4 on = o[4] * rcp(l[4][0]); Op[4] = v2u{pk(on[0], on[1]), pk(on[2], on[3])}; }
  const unsigned ob = W_O + L * BIG_L + (x.c * 64 + 4 * x.hg + x.w) * 1024;
  stc4(x.rS, x.lane * 16, v4u{Op[1].x, Op[1].y, Op[2].x, Op[2].y}, ob);
  stc4(x.rS, x.lane * 16, v4u{Op[3].x, Op[3].y, Op[4].x, Op[4].y}, ob + 32 * 1024);
}

DEV void top2(const Ctx& x, int L, unsigned& k1, unsigned& k2, bool nap) {
  if (x.w == 0) {
    v2u k;
    for (int sp = 0; sp < SPIN; ++sp) {
      MEMF();
      k = ldc2(x.rS, x.lane * 128, W_KEY + L * KEY_L);
      if (__all(k.x != POISON && k.y != POISON)) break;
      if (nap) __builtin_amdgcn_s_sleep(4);
    }
    const unsigned m1 = x32maxu(x16maxu(rowmaxu(k.x)));
    const unsigned m2 = x32maxu(x16maxu(rowmaxu(k.x == m1 ? k.y : k.x)));
    *lp<v2u>(M_TOP) = v2u{m1, m2};
  }
  BAR();
  const v2u k = *lp<v2u>(M_TOP);
  k1 = rfl(k.x); k2 = rfl(k.y);
}
DEV int key_tok(unsigned k) { return 2047 - (int)(k & 0x7FFu); }
DEV float key_sig(unsigned k) {
  unsigned u = (k & ~0x7FFu) | 0x400u;
  u = (u & 0x80000000u) ? (u ^ 0x80000000u) : ~u;
  return rcp(1.f + ex2(-LOG2E * f_(u)));
}

struct Ffn { v4u GU[4][4]; v4u WD[8]; float cs[4]; };
DEV void ffn_load_down(const Ctx& x, rsrc_t rD, v4u (&raw)[8]) {
#pragma unroll
  for (int e = 0; e < 8; ++e) raw[e] = ldp4(rD, (8 * x.g + e) * 1024 + x.t * 16, x.ff * 32768 + x.w * 256);
}
DEV void ffn_prep(const Ctx& x, const v4u (&raw)[8], Ffn& F) {
  const int qq = x.t >> 2, p = x.lane & 3;
  const unsigned b0 = RING + (128 * x.w + 8 * x.g + qq) * 64 + (x.g & 1) * 32 + 8 * p, b1 = b0 ^ 32;
  const v4u ones = {ONE2, ONE2, ONE2, ONE2};
#pragma unroll
  for (int c = 0; c < 8; ++c)
#pragma unroll
    for (int d = 0; d < 4; ++d) F.WD[c][d] = __builtin_amdgcn_perm(raw[2 * d + 1][c >> 1], raw[2 * d][c >> 1], (c & 1) ? 0x07060302u : 0x05040100u);
  TIE0_4(F.WD[0], F.WD[1], F.WD[2], F.WD[3]); TIE0_4(F.WD[4], F.WD[5], F.WD[6], F.WD[7]);
  SB();
  f32x4 cs[4];
#pragma unroll
  for (int s = 0; s < 4; ++s)
#pragma unroll
    for (int m = 0; m < 4; ++m) {
      const unsigned o = ((m & 1) ? b1 : b0) + s * 2048 + (m >> 1) * 32768;
      F.GU[s][m] = trd2(o, o + 256);
    }
#pragma unroll
  for (int s = 0; s < 4; ++s)
#pragma unroll
    for (int m = 0; m < 4; ++m) { if (s == 0) mf_vvn<true>(cs[m], ones, F.GU[s][m]); else mf_vv<false>(cs[m], ones, F.GU[s][m]); }
  TIE4(7, cs[0], cs[1], cs[2], cs[3]);
#pragma unroll
  for (int m = 0; m < 4; ++m) F.cs[m] = cs[m][0];
}
DEV void ffn_run(const Ctx& x, const Ffn& F, int L, unsigned k1, unsigned k2) {
  const int ta = key_tok(k1), tb = key_tok(k2);
  const int tk = ((x.t >> 2) & 1) ? tb : ta;
  const unsigned xo = ((tk >> 4) * 16 + 4 * x.w) * 1024 + (16 * x.g + (tk & 15)) * 16;
  v4u xf[4]; v2u sa, sb;
  for (int sp = 0; sp < SPIN; ++sp) {
    MEMF();
#pragma unroll
    for (int s = 0; s < 4; ++s) xf[s] = ldc4(x.rS, xo + s * 1024, W_X + L * BIG_L);
    sa = ldc2(x.rS, ta * 8, W_ST + L * ST_L); sb = ldc2(x.rS, tb * 8, W_ST + L * ST_L);
    const unsigned m = max(max(max(xf[0].x, xf[1].x), max(xf[2].x, xf[3].x)), max(sa.x, sb.x));
    if (__all(m != POISON)) break;
  }
  f32x4 d[4];
#pragma unroll
  for (int s = 0; s < 4; ++s)
#pragma unroll
    for (int m = 0; m < 4; ++m) { if (s == 0) mf_vvn<true>(d[m], xf[s], F.GU[s][m]); else mf_vv<false>(d[m], xf[s], F.GU[s][m]); }
  TIE4(7, d[0], d[1], d[2], d[3]);
  const float mt = (x.g & 1) ? f_(sb.x) : f_(sa.x);
  const bool hi = x.g >= 2;
  const float vg = (hi ? d[1][0] : d[0][0]) - mt * (hi ? F.cs[1] : F.cs[0]);
  const float vu = (hi ? d[3][0] : d[2][0]) - mt * (hi ? F.cs[3] : F.cs[2]);
  const unsigned ro = M_RED + ((x.g & 1) * 32 + 16 * (x.g >> 1) + x.t) * 16 + 4 * x.w;
  *lp<float>(ro) = vg; *lp<float>(ro + 1024) = vu;
  BAR();
  const f32x4 g4 = *lp<f32x4>(M_RED + x.lane * 16), u4 = *lp<f32x4>(M_RED + 1024 + x.lane * 16);
  const float rs = rsq((hi ? f_(sb.y) : f_(sa.y)) + EPS), sg = hi ? key_sig(k2) : key_sig(k1);
  const float gv = ((g4[0] + g4[1]) + (g4[2] + g4[3])) * rs, uv = ((u4[0] + u4[1]) + (u4[2] + u4[3])) * rs;
  const float h = gv * rcp(1.f + ex2(-LOG2E * gv)) * uv * sg;
  *lp<u16>(M_HF + x.w * 128 + x.lane * 2) = (u16)pk(h, h);
  const v4u hf = *lp<v4u>(M_HF + x.w * 128 + ((x.t >> 2) & 1) * 64 + x.g * 16);
  f32x4 y4[8];
#pragma unroll
  for (int c = 0; c < 8; ++c) mf_vv<true>(y4[c], hf, F.WD[c]);
  TIE4(3, y4[0], y4[1], y4[2], y4[3]); TIE4(3, y4[4], y4[5], y4[6], y4[7]);
  float y[8];
#pragma unroll
  for (int c = 0; c < 8; ++c) y[c] = y4[c][0];
  const unsigned yo = hi ? OOB : ((x.g & 1) * 44 + x.ff) * 1024 + x.w * 256 + x.t * 16;
  stc4(x.rS, yo, v4u{pk(y[0], y[1]), pk(y[2], y[3]), pk(y[4], y[5]), pk(y[6], y[7])}, W_Y + L * Y_L);
}
DEV v4u sel_frag(int t) {
  const unsigned v = (t & 1) ? 0x3F800000u : 0x00003F80u;
  return v4u{(t >> 1) == 0 ? v : 0u, (t >> 1) == 1 ? v : 0u, (t >> 1) == 2 ? v : 0u, (t >> 1) == 3 ? v : 0u};
}
DEV void y_slice(const Ctx& x, int L, int slot) {
  const v4u sel = sel_frag(x.t);
  const int p0 = 4 * (x.t >> 2) + x.g;
  const unsigned o = (slot * 44 + p0) * 1024 + (128 * x.q + 32 * x.w) * 2 + (x.t & 3) * 16;
  f32x4 d;
  for (int sp = 0; sp < SPIN; ++sp) {
    MEMF();
    const v4u a = ldc4(x.rS, o, W_Y + L * Y_L), b = ldc4(x.rS, o + 16384, W_Y + L * Y_L), c = ldc4(x.rS, p0 < NFF - 32 ? o + 32768 : OOB, W_Y + L * Y_L);
    mf_vvn<true>(d, sel, a); mf_vvn<false>(d, sel, b); mf_vvn<false>(d, sel, c);
    TIE1(7, d);
    if (__all(max(max(a.x, b.x), c.x) != POISON)) break;
  }
#pragma unroll
  for (int r = 0; r < 4; ++r) { d[r] += dppf<0x124>(d[r]); d[r] += dppf<0x128>(d[r]); }
  if (x.g < 2) *lp<f32x4>(M_YS + x.w * 128 + ((x.t & 3) * 8 + 4 * x.g) * 4) = d;
}

DEV void outproj(Ctx& x, const v4u (&WO)[8][2], const v4u (&ob)[8], f32x4 (&acc)[2][2], const v4u wfr, const float wsum, int L, v4u (&xk)[2]) {
  const int w = x.w;
#pragma unroll
  for (int j = 0; j < 4; ++j) {
    const v4u a = ob[2 * j], b = ob[2 * j + 1];
    *lp<v4u>(XIMG + (4 * w + j) * 1024 + x.lane * 16) = v4u{a.x, a.y, b.x, b.y};
    *lp<v4u>(XIMG + (16 + 4 * w + j) * 1024 + x.lane * 16) = v4u{a.z, a.w, b.z, b.w};
  }
  BAR();
  STAMPI(7 + 20 * L);
  {
    v4u of[3][2];
#pragma unroll
    for (int j = 0; j < 2; ++j) { of[j][0] = *lp<v4u>(XIMG + j * 1024 + x.lane * 16); of[j][1] = *lp<v4u>(XIMG + (16 + j) * 1024 + x.lane * 16); }
    TIE4(1, acc[0][0], acc[0][1], acc[1][0], acc[1][1]);
    sfor<0, 32>([&](auto J) {
      constexpr int j = J.v >> 1, T = J.v & 1;
      if constexpr (j < 8) mf_av<false, AO(j, 0)>(acc[T][0], of[j % 3][T]); else mf_vv<false>(acc[T][0], WO[j - 8][0], of[j % 3][T]);
      if constexpr (j + 2 < 16) of[(j + 2) % 3][T] = *lp<v4u>(XIMG + (16 * T + j + 2) * 1024 + x.lane * 16);
      SB();
      if constexpr (j < 8) mf_av<false, AO(j, 1)>(acc[T][1], of[j % 3][T]); else mf_vv<false>(acc[T][1], WO[j - 8][1], of[j % 3][T]);
      SB();
    });
    TIE4(7, acc[0][0], acc[0][1], acc[1][0], acc[1][1]);
  }
  STAMPI(8 + 12 * L);
#pragma unroll
  for (int T = 0; T < 2; ++T) {
    xk[T] = v4u{pk(acc[T][0][0], acc[T][0][1]), pk(acc[T][0][2], acc[T][0][3]), pk(acc[T][1][0], acc[T][1][1]), pk(acc[T][1][2], acc[T][1][3])};
    stc4(x.rS, x.lane * 16, xk[T], W_X + L * BIG_L + ((4 * x.c2 + 2 * x.u + T) * 16 + 4 * x.q + w) * 1024);
  }
  f32x4 dd[2];
  mf_vvn<true>(dd[0], wfr, xk[0]); mf_vvn<true>(dd[1], wfr, xk[1]);
  TIE2(7, dd[0], dd[1]);
#pragma unroll
  for (int T = 0; T < 2; ++T) {
    const f32x4 d = dd[T];
    float s2 = dot2(xk[T].x, xk[T].x, dot2(xk[T].y, xk[T].y, 0.f)) + dot2(xk[T].z, xk[T].z, dot2(xk[T].w, xk[T].w, 0.f));
    s2 = x32sum(x16sum(s2));
    if (x.g == 0) *lp<f32x4>(M_RED + (w * 32 + 16 * T + x.t) * 16) = f32x4{d[0], d[1], s2, 0.f};
  }
  BAR();
  if (w == 0) {
    const int tk = x.lane & 31, hf = x.lane >> 5;
    const unsigned pb = W_PST + L * PST_L + (x.c2 * 2 + x.u) * 2048;
    {
      const f32x4 a = *lp<f32x4>(M_RED + (hf * 64 + tk) * 16) + *lp<f32x4>(M_RED + (hf * 64 + 32 + tk) * 16);
      const f32x4 s = {x32sum(a[0]), x32sum(a[1]), x32sum(a[2]), 0.f};
      stc4(x.rS, hf ? OOB : x.q * 512 + tk * 16, __builtin_bit_cast(v4u, s), pb);
    }
    STAMPI(57 + 3 * L);
    DRAIN();
    STAMPI(58 + 3 * L);
    v4u a, b;
    for (int sp = 0; sp < SPIN; ++sp) {
      MEMF();
      a = ldc4(x.rS, hf * 1024 + tk * 16, pb); b = ldc4(x.rS, hf * 1024 + 512 + tk * 16, pb);
      if (__all(a.w != POISON && b.w != POISON)) break;
    }
    STAMPI(59 + 3 * L);
    const float s1 = x32sum(f_(a.x) + f_(b.x)), sw = x32sum(f_(a.y) + f_(b.y)), s2 = x32sum(f_(a.z) + f_(b.z));
    const float m = s1 * (1.f / 512), var = fmaxf(s2 * (1.f / 512) - m * m, 0.f), rs = rsq(var + EPS);
    if (x.q == 0) {
      const float r = rs * (sw - m * wsum);
      unsigned k = u_(r); k = (k & 0x80000000u) ? ~k : (k | 0x80000000u);
      const int tok = x.c2 * 64 + x.u * 32 + tk;
      k = (k & ~0x7FFu) | (unsigned)(2047 - tok);
      const unsigned m1 = x16maxu(rowmaxu(k)), m2 = x16maxu(rowmaxu(k == m1 ? 0u : k));
      stc2(x.rS, x.lane == 0 ? (x.c2 * 2 + x.u) * 128 : OOB, v2u{m1, m2}, W_KEY + L * KEY_L);
      stc2(x.rS, hf ? OOB : tok * 8, v2u{u_(m), u_(var)}, W_ST + L * ST_L);
    }
    *lp<f32x2>(M_PS + tk * 8) = f32x2{m, rs};
  }
}
DEV void load_o(const Ctx& x, int L, v4u (&ob)[8]) {
  const unsigned so = W_O + L * BIG_L + ((x.c2 * 2 + x.u) * 32 + 8 * x.w) * 1024;
  for (int sp = 0; sp < SPIN; ++sp) {
    MEMF();
#pragma unroll
    for (int i = 0; i < 8; ++i) ob[i] = ldc4(x.rS, x.lane * 16 + i * 1024, so);
    const unsigned m = max(max(max(ob[0].x, ob[1].x), max(ob[2].x, ob[3].x)), max(max(ob[4].x, ob[5].x), max(ob[6].x, ob[7].x)));
    if (__all(m != POISON)) break;
  }
}
DEV void unpack8(v4u v, f32x4& a, f32x4& b) { a = f32x4{blo(v.x), bhi(v.x), blo(v.y), bhi(v.y)}; b = f32x4{blo(v.z), bhi(v.z), blo(v.w), bhi(v.w)}; }

__global__ void __launch_bounds__(256, 1)
fused(const u16* __restrict__ x0, const u16* __restrict__ wqkv, const u16* __restrict__ wo, const u16* __restrict__ wr, const u16* __restrict__ wg,
      const u16* __restrict__ wu, const u16* __restrict__ wd, u16* __restrict__ out, char* __restrict__ ws) {
  Ctx x;
  const int b = blockIdx.x, tid = threadIdx.x, lane = tid & 63, w = __builtin_amdgcn_readfirstlane(tid >> 6), t = lane & 15, g = lane >> 4;
  const int xc = b & 7, jb = b >> 3;
  x.lane = lane; x.w = w; x.t = t; x.g = g;
  x.c = xc * 4 + (jb >> 3); x.hg = jb & 7; x.c2 = x.c; x.u = x.hg >> 2; x.q = x.hg & 3;
  { const int f = (jb / 6) * 8 + xc; x.ff = (jb % 6 == 1 && f < NFF) ? f : -1; }
  const int c = x.c, hg = x.hg;
  x.rS = mk_rs(ws, W_END);
  const v4u sS = mk_srd(ws, W_END), sX = mk_srd(x0, 2048 * 1024), sW = mk_srd(wqkv, 512 * 3072), sO = mk_srd(wo, 512 * 1024);
  u64 t_a = 0, t_b = 0;
  CLAIM_AGPRS();
  STAMP(0);

  v2u ep;
  asm volatile("buffer_load_dwordx2 %0, off, %1, 0 sc1" : "=v"(ep) : "s"(sS) : "memory");
  sfor<0, 20>([&](auto I) {
    constexpr int i = I.v;
    const int R = 20 * w + i, tok = (c == 0 && R < 16) ? R : 64 * c - 16 + R;
    dma<i * 1024, 0>(sX, (lane ^ (R & 15)) * 16, XIMG + 20 * w * 1024, tok * 1024);
  });
  const unsigned wvo = (8 * w + (lane >> 3)) * 3072 + hg * 128 + (((((lane & 7) >> 1) ^ (((lane >> 4) & 1) + 2 * (w & 1))) * 2) + (lane & 1)) * 16;
  const unsigned ringw = RING + w * 1024;
  auto issue_w = [&](auto S) {
    constexpr int s = S.v;
    sfor<0, 3>([&](auto P) { dma<(s % 6) * 12288 + P.v * 4096, s * 98304 + P.v * 1024>(sW, wvo, ringw, 0); });
  };
  const unsigned ovo = g * 1024 + x.q * 256 + (t ^ (4 * g)) * 16;
  auto issue_o_ring = [&](auto S) {
    constexpr int sl = S.v;
    sfor<0, 3>([&](auto P) { dma<(12 * sl + P.v) * 1024, (12 * sl + P.v) * 4096>(sO, ovo, RING + 3 * w * 1024, 3 * w * 4096); });
  };
  const v4u pz = {POISON, POISON, POISON, POISON};
  sfor<0, 5>([&](auto I) {
    constexpr int i = I.v;
    waitv<16 + i>();
    STAMP(50 + i);
    if constexpr (i < 2) {
      const unsigned ob = W_O + i * BIG_L + (c * 64 + 4 * hg + w) * 1024;
      stc4(x.rS, lane * 16, pz, ob); stc4(x.rS, lane * 16, pz, ob + 32768);
    } else if constexpr (i < 4) {
      const unsigned xb = W_X + (i - 2) * BIG_L + ((4 * x.c2 + 2 * x.u) * 16 + 4 * x.q + w) * 1024;
      stc4(x.rS, lane * 16, pz, xb); stc4(x.rS, lane * 16, pz, xb + 16384);
    } else {
      const int gi = x.c2 * 2 + x.u;
      unsigned so = OOB;
      if (w == 1) so = W_PST + (lane >> 5) * PST_L + gi * 2048 + x.q * 512 + (lane & 31) * 16;
      if (w == 2 && x.q == 0) so = lane < 32 ? W_ST + g * ST_L + gi * 256 + t * 16 : (lane < 34 ? W_KEY + (lane - 32) * KEY_L + gi * 128 : OOB);
      stc4(x.rS, so, pz);
      stc4(x.rS, x.ff >= 0 ? W_Y + (w >> 1) * Y_L + ((w & 1) * 44 + x.ff) * 1024 + lane * 16 : OOB, pz);
    }
    issue_w(I);
    const int R = 20 * w + 4 * i + g;
    const unsigned ra = XIMG + R * 1024 + t * 16;
    float s1a = 0.f, s1b = 0.f, s2a = 0.f, s2b = 0.f;
#pragma unroll
    for (int k = 0; k < 4; ++k) {
      const v4u v = *lp<v4u>(ra + k * 256);
      s1a = dot2(v.x, ONE2, s1a); s2a = dot2(v.x, v.x, s2a); s1b = dot2(v.y, ONE2, s1b); s2b = dot2(v.y, v.y, s2b);
      s1a = dot2(v.z, ONE2, s1a); s2a = dot2(v.z, v.z, s2a); s1b = dot2(v.w, ONE2, s1b); s2b = dot2(v.w, v.w, s2b);
    }
    const float m = rowsum(s1a + s1b) * (1.f / 512), var = fmaxf(rowsum(s2a + s2b) * (1.f / 512) - m * m, 0.f);
    *lp<float>(M_STM + R * 4) = m; *lp<float>(M_STA + R * 4) = rsq(var + EPS);
    if (i == 0 && w == 0) *lp<v4u>(XIMG + lane * 16) = v4u{ONE2, ONE2, ONE2, ONE2};
    SB();
  });
  asm volatile("" : "+v"(ep));
  const unsigned e0 = rfl(ep.x) + 1u, e1 = rfl(ep.y);
  STAMP(1);

  Acc a;
  {
    unsigned xb[4];
#pragma unroll
    for (int k = 0; k < 4; ++k) xb[k] = XIMG + t * 1024 + (((4 * k) ^ (t & 12)) | (g ^ (t & 3))) * 16;
    const int qq = t >> 2;
    const unsigned wb = RING + (8 * g + qq) * 128 + (w ^ ((qq >> 1) + 2 * (g & 1))) * 32 + 8 * (lane & 3);
    const unsigned wb2 = wb + 36864;
    v4u xf[2][5];
    sfor<0, 17>([&](auto S) {
      constexpr int s = S.v;
      if constexpr (s < 16) {
        waitv<(s < 4 ? 20 - 2 * s : 12)>();
        BAR();
        STAMP(32 + s);
      }
      auto mm = [&](auto N) {
        constexpr int n = N.v;
        if constexpr (s > 0) {
          constexpr int i = n / 3, p = n % 3, z = (s - 1) & 1;
          if constexpr (p == 0) mf_av<s == 1, AW(s - 1, 1)>(a.K[i], xf[z][i]);
          if constexpr (p == 1) mf_va<s == 1, AW(s - 1, 2)>(a.V[i], xf[z][i]);
          if constexpr (p == 2) mf_av<s == 1, AW(s - 1, 0)>(a.Q[i], xf[z][i]);
        }
        SB();
      };
      sfor<0, 15>([&](auto N) {
        constexpr int n = N.v;
        mm(N);
        if constexpr (s < 16) {
          if constexpr (n < 3) {
            if constexpr (s + 5 < 16) dma<((s + 5) % 6) * 12288 + n * 4096, (s + 5) * 98304 + n * 1024>(sW, wvo, ringw, 0);
            else dma<(12 * ((s + 5) % 6) + n) * 1024, (12 * ((s + 5) % 6) + n) * 4096>(sO, ovo, RING + 3 * w * 1024, 3 * w * 4096);
          } else if constexpr (n < 6) {
            constexpr int o = (s % 3) * 12288 + (n - 3) * 4096;
            trd_a<AW(s, n - 3), o, o + 512>((s % 6) < 3 ? wb : wb2);
          } else if constexpr (n < 11) {
            xf[s & 1][n - 6] = *lp<v4u>(xb[s & 3] + (n - 6) * 16384 + (s >> 2) * 256);
          } else if constexpr (n == 11 && s == 4) {
            stc2(x.rS, lane == 0 ? W_FLAGS + (b * 4 + w) * 8 : OOB, v2u{e0, e1});
          }
          SB();
        }
      });
      if constexpr (s == 15) {
        BAR();
      }
    });
  }
  TIE3(7, a.Q[4], a.K[4], a.V[4]);
#pragma unroll
  for (int i = 0; i < 4; ++i) asm volatile("" : "+v"(a.Q[i]), "+v"(a.K[i]), "+v"(a.V[i]));
  STAMP(2);
  const rsrc_t rX = mk_rs(x0, 2048 * 1024), rR = mk_rs(wr, 1024);
  const int n0 = 128 * x.q + 32 * w;
  v4u xk[2];
#pragma unroll
  for (int T = 0; T < 2; ++T) xk[T] = ldp4(rX, (16 * T + t) * 1024 + g * 16, (64 * x.c2 + 32 * x.u) * 1024 + n0 * 2);
  const v4u wr8 = ldp4(rR, g * 16, n0 * 2), wrall = ldp4(rR, lane * 16);
  issue_o_ring(IC<3>{});
  sfor<0, 14>([&](auto K) { dma<K.v * 1024, K.v * 4096>(sO, ovo, XIMG + 24576 + 14 * w * 1024, (72 + 14 * w) * 4096); });
  v4u fl0 = ldc4(x.rS, lane * 16, W_FLAGS + w * 2048), fl1 = ldc4(x.rS, lane * 16 + 1024, W_FLAGS + w * 2048);
  SB();
  STAMP(56);
  if (t == 0) { *lp<f32x4>(M_CS + w * 192 + g * 16) = a.Q[0]; *lp<f32x4>(M_CS + w * 192 + 64 + g * 16) = a.K[0]; }
  if (g == 0) *lp<float>(M_CS + w * 192 + 128 + t * 4) = a.V[0][0];
  attention(x, a, 0);
  STAMP(3);
  v4u WO[8][2];
  const unsigned obs = (4 * g + (t >> 2)) * 256 + ((4 * w + (lane & 3)) ^ (t & 12)) * 16;
  waitv<18>();
  BAR();
  sfor<0, 16>([&](auto J) { constexpr int j = J.v >> 1, ct = J.v & 1; trd_a<AO(j, ct), j * 8192 + 8 * ct, j * 8192 + 4096 + 8 * ct>(RING + obs); });
#pragma unroll
  for (int ct = 0; ct < 2; ++ct) WO[0][ct] = trd2(RING + 65536 + obs + 8 * ct, RING + 65536 + obs + 4096 + 8 * ct);
  SB();
  DRAIN();
  for (int sp = 0; sp < SPIN; ++sp) {
    if (__all(fl0.x == e0 && fl0.y == e1 && fl0.z == e0 && fl0.w == e1 && fl1.x == e0 && fl1.y == e1 && fl1.z == e0 && fl1.w == e1)) break;
    MEMF();
    fl0 = ldc4(x.rS, lane * 16, W_FLAGS + w * 2048); fl1 = ldc4(x.rS, lane * 16 + 1024, W_FLAGS + w * 2048);
  }
  BAR();
  STAMP(4);
  if (b == 0 && w == 3) stc2(x.rS, lane == 0 ? W_EPOCH : OOB, v2u{e0, e1});
  v4u ob[8];
  {
    const unsigned so = W_O + ((x.c2 * 2 + x.u) * 32 + 8 * w) * 1024;
#pragma unroll
    for (int i = 0; i < 8; ++i) ob[i] = ldc4(x.rS, lane * 16 + i * 1024, so);
    SB();
#pragma unroll
    for (int j = 9; j < 16; ++j)
#pragma unroll
      for (int ct = 0; ct < 2; ++ct) WO[j - 8][ct] = trd2(XIMG + 24576 + obs + (j - 9) * 8192 + 8 * ct, XIMG + 24576 + obs + (j - 9) * 8192 + 4096 + 8 * ct);
    SB();
    const unsigned m = max(max(max(ob[0].x, ob[1].x), max(ob[2].x, ob[3].x)), max(max(ob[4].x, ob[5].x), max(ob[6].x, ob[7].x)));
    if (!__all(m != POISON)) load_o(x, 0, ob);
  }
  STAMP(5);
  const v4u wfr = t == 0 ? v4u{ONE2, ONE2, ONE2, ONE2} : (t == 1 ? wr8 : v4u{0u, 0u, 0u, 0u});
  const float wsum = x32sum(x16sum(rowsum(dot2(wrall.x, ONE2, dot2(wrall.y, ONE2, 0.f)) + dot2(wrall.z, ONE2, dot2(wrall.w, ONE2, 0.f)))));
  f32x4 acc[2][2];
#pragma unroll
  for (int T = 0; T < 2; ++T) unpack8(xk[T], acc[T][0], acc[T][1]);
  BAR();
  if (x.ff >= 0) {
    const v4u sG = mk_srd(wg, 512 * 2752), sU = mk_srd(wu, 512 * 2752);
    const unsigned gvo = (lane >> 2) * 2752 + x.ff * 64 + (((((lane >> 1) & 1) ^ (lane >> 5)) * 2) + (lane & 1)) * 16;
    sfor<0, 8>([&](auto K) {
      dma<K.v * 1024, K.v * 16 * 2752>(sG, gvo, RING + w * 8192, w * 128 * 2752);
      dma<32768 + K.v * 1024, K.v * 16 * 2752>(sU, gvo, RING + w * 8192, w * 128 * 2752);
    });
  }
  outproj(x, WO, ob, acc, wfr, wsum, 0, xk);
  STAMP(9);
  const rsrc_t rD = mk_rs(wd, 1376 * 1024);
  if (w == 1) {
    DRAIN();
    const int gi = max(2 * c - 1 + (t >> 2), 0);
    const unsigned o = t < 12 ? gi * 2048 + (t & 3) * 512 + 12 : OOB;
    for (int sp = 0; sp < SPIN; ++sp) {
      MEMF();
      if (__all(ldc1(x.rS, o, W_PST) != POISON)) break;
    }
  }
  BAR();
  STAMP(10);

  unsigned k1 = 0, k2 = 0;
  auto issue_x1 = [&]() {
#pragma unroll
    for (int k = 0; k < 5; ++k) {
      const int n = 20 * w + 4 * k;
      dma4_sc1(sS, lane * 16, XIMG + n * 1024, W_X + ((4 * c - 1) * 16 + n + ((c == 0 && n < 16) ? 16 : 0)) * 1024);
    }
  };
  issue_x1();
  if (x.ff >= 0) {
    v4u raw[8]; Ffn F;
    ffn_load_down(x, rD, raw);
    DRAIN();
    ffn_prep(x, raw, F);
    top2(x, 0, k1, k2, false);
    STAMP(11);
    ffn_run(x, F, 0, k1, k2);
    STAMP(12);
  }
#pragma unroll 1
  for (int tries = 0; tries < 64; ++tries) {
    DRAIN();
    BAR();
    STAMP(28);
    v4u xf[2][5];
#pragma unroll
    for (int i = 0; i < 5; ++i) xf[0][i] = *lp<v4u>(XIMG + i * 16384 + lane * 16);
    sfor<0, 16>([&](auto S) {
      constexpr int s = S.v;
      sfor<0, 5>([&](auto I) {
        constexpr int i = I.v;
        mf_av<s == 0, AW(s, 1)>(a.K[i], xf[s & 1][i]);
        if constexpr (s < 15) xf[(s + 1) & 1][i] = *lp<v4u>(XIMG + (i * 16 + s + 1) * 1024 + lane * 16);
        SB();
        mf_va<s == 0, AW(s, 2)>(a.V[i], xf[s & 1][i]);
        if constexpr (i > 0) mf_av<s == 0, AW(s, 0)>(a.Q[i], xf[s & 1][i]);
        SB();
      });
    });
    TIE3(7, a.Q[4], a.K[4], a.V[4]);
#pragma unroll
    for (int i = 1; i < 4; ++i) asm volatile("" : "+v"(a.Q[i]), "+v"(a.K[i]), "+v"(a.V[i]));
    TIE0_2(a.K[0], a.V[0]);
    const float chk = (a.K[0][0] + a.K[1][0]) + (a.K[2][0] + a.K[3][0]) + a.K[4][0];
    if (!__any(isnan_(chk))) break;
    BAR();
    issue_x1();
  }
  STAMP(13);
  if (w == 1) {
    v2u sa, sb;
    const int ta = 64 * c - 16 + lane;
    for (int sp = 0; sp < SPIN; ++sp) {
      MEMF();
      sa = ldc2(x.rS, max(ta, 0) * 8, W_ST); sb = ldc2(x.rS, (64 * c + 48 + t) * 8, W_ST);
      if (__all(sa.x != POISON && sb.x != POISON)) break;
    }
    const float ra = 1.f + rsq(f_(sa.y) + EPS), rb = 1.f + rsq(f_(sb.y) + EPS);
    *lp<float>(M_STM + lane * 4) = f_(sa.x); *lp<float>(M_STA + lane * 4) = ra * rsq(f_(sa.y) * ra * ra + EPS);
    *lp<float>(M_STM + (64 + t) * 4) = f_(sb.x); *lp<float>(M_STA + (64 + t) * 4) = rb * rsq(f_(sb.y) * rb * rb + EPS);
  }
  if (x.ff < 0) top2(x, 0, k1, k2, false); else BAR();
  STAMP(14);
#pragma unroll 1
  for (int sl = 0; sl < 2; ++sl) {
    const int tau = key_tok(sl ? k2 : k1), R = tau - (64 * c - 16);
    if (R < 2 || R >= 80 || (c == 0 && R < 16)) continue;
    const v4u sel = sel_frag(t);
    const unsigned o = (sl * 44 + g) * 1024 + w * 256 + t * 16;
    const v2u xr = *lp<v2u>(XIMG + ((R >> 4) * 16 + 4 * w + (t >> 2)) * 1024 + ((t & 3) * 16 + (R & 15)) * 16 + (g & 1) * 8);
    const v2u st = ldc2(x.rS, tau * 8, W_ST);
    f32x4 d;
    for (int sp = 0; sp < SPIN; ++sp) {
      MEMF();
      v4u y[11];
#pragma unroll
      for (int i = 0; i < 11; ++i) y[i] = ldc4(x.rS, (i == 10 && g == 3) ? OOB : o + i * 4096, W_Y);
      unsigned m = 0;
#pragma unroll
      for (int i = 0; i < 11; ++i) { if (i == 0) mf_vvn<true>(d, sel, y[i]); else mf_vv<false>(d, sel, y[i]); m = max(m, y[i].x); }
      TIE1(7, d);
      if (__all(m != POISON)) break;
    }
    STAMP(15);
    const float m1 = f_(st.x), rs1 = rsq(f_(st.y) + EPS);
    const f32x4 xv = {blo(xr.x), bhi(xr.x), blo(xr.y), bhi(xr.y)};
    const f32x4 x2 = xv * (1.f + rs1) - m1 * rs1 + d;
    const v2u xp = {pk(x2[0], x2[1]), pk(x2[2], x2[3])};
    float s1 = dot2(xp.x, ONE2, dot2(xp.y, ONE2, 0.f)), s2 = dot2(xp.x, xp.x, dot2(xp.y, xp.y, 0.f));
    s1 = x16sum(rowsum(s1)); s2 = x16sum(rowsum(s2));
    if (g < 2) { *lp<v2u>(M_FIX + w * 256 + t * 16 + g * 8) = xp; *lp<f32x2>(M_FST + w * 8) = f32x2{s1, s2}; }
    BAR();
    f32x4 fq, fk, fv;
    {
      v4u xf[4];
      const unsigned fb = M_FIX + g * 16;
#pragma unroll
      for (int s = 0; s < 3; ++s) xf[s] = *lp<v4u>(fb + s * 64);
      sfor<0, 16>([&](auto S) {
        constexpr int s = S.v;
        mf_av<s == 0, AW(s, 0)>(fq, xf[s & 3]);
        if constexpr (s + 3 < 16) xf[(s + 3) & 3] = *lp<v4u>(fb + (s + 3) * 64);
        SB();
        mf_av<s == 0, AW(s, 1)>(fk, xf[s & 3]); mf_va<s == 0, AW(s, 2)>(fv, xf[s & 3]);
        SB();
      });
      TIE3(7, fq, fk, fv);
    }
    const f32x4 sa = *lp<f32x4>(M_FST), sb = *lp<f32x4>(M_FST + 16);
    const float m2 = ((sa[0] + sa[2]) + (sb[0] + sb[2])) * (1.f / 512);
    const float v2 = fmaxf(((sa[1] + sa[3]) + (sb[1] + sb[3])) * (1.f / 512) - m2 * m2, 0.f);
    const bool mt = t == (R & 15);
#pragma unroll
    for (int i = 0; i < 5; ++i)
      if ((R >> 4) == i) {
#pragma unroll
        for (int r = 0; r < 4; ++r) {
          if (i > 0) a.Q[i][r] = mt ? fq[r] : a.Q[i][r];
          a.K[i][r] = mt ? fk[r] : a.K[i][r];
          a.V[i][r] = (4 * g + r == (R & 15)) ? fv[r] : a.V[i][r];
        }
      }
    *lp<float>(M_STM + R * 4) = m2; *lp<float>(M_STA + R * 4) = rsq(v2 + EPS);
    BAR();
  }
  STAMP(16);
  attention(x, a, 1);
  STAMP(17);
#pragma unroll
  for (int T = 0; T < 2; ++T) {
    const f32x2 ps = *lp<f32x2>(M_PS + (16 * T + t) * 8);
    unpack8(xk[T], acc[T][0], acc[T][1]);
    acc[T][0] = acc[T][0] * (1.f + ps[1]) - ps[0] * ps[1]; acc[T][1] = acc[T][1] * (1.f + ps[1]) - ps[0] * ps[1];
  }
#pragma unroll 1
  for (int sl = 0; sl < 2; ++sl) {
    const int R = key_tok(sl ? k2 : k1) - (64 * x.c2 + 32 * x.u);
    if (R < 0 || R >= 32) continue;
    y_slice(x, 0, sl);
    const f32x4 ya = *lp<f32x4>(M_YS + w * 128 + g * 32), yb = *lp<f32x4>(M_YS + w * 128 + g * 32 + 16);
#pragma unroll
    for (int T = 0; T < 2; ++T) { const float f = (16 * T + t == R) ? 1.f : 0.f; acc[T][0] += ya * f; acc[T][1] += yb * f; }
  }
  DRAIN();
  STAMP(18);
  load_o(x, 1, ob);
  STAMP(19);
  BAR();
  outproj(x, WO, ob, acc, wfr, wsum, 1, xk);
  STAMP(21);
  BAR();
  const rsrc_t rOut = mk_rs(out, 2048 * 1024);
  const unsigned oso = (64 * x.c2 + 32 * x.u) * 1024 + n0 * 2;
#pragma unroll
  for (int T = 0; T < 2; ++T) {
    const f32x2 ps = *lp<f32x2>(M_PS + (16 * T + t) * 8);
    unpack8(xk[T], acc[T][0], acc[T][1]);
    acc[T][0] = acc[T][0] * (1.f + ps[1]) - ps[0] * ps[1]; acc[T][1] = acc[T][1] * (1.f + ps[1]) - ps[0] * ps[1];
    __builtin_amdgcn_raw_buffer_store_b128(v4u{pk(acc[T][0][0], acc[T][0][1]), pk(acc[T][0][2], acc[T][0][3]), pk(acc[T][1][0], acc[T][1][1]), pk(acc[T][1][2], acc[T][1][3])},
                                           rOut, (16 * T + t) * 1024 + g * 16, oso, 0);
  }
  STAMP(22);
  if (x.ff >= 0) {
    v4u raw[8]; Ffn F;
    ffn_load_down(x, rD, raw);
    ffn_prep(x, raw, F);
    top2(x, 1, k1, k2, false);
    STAMP(23);
    ffn_run(x, F, 1, k1, k2);
    STAMP(24);
  } else top2(x, 1, k1, k2, true);
  STAMP(25);
#pragma unroll 1
  for (int sl = 0; sl < 2; ++sl) {
    const int R = key_tok(sl ? k2 : k1) - (64 * x.c2 + 32 * x.u);
    if (R < 0 || R >= 32) continue;
    y_slice(x, 1, sl);
    const f32x4 ya = *lp<f32x4>(M_YS + w * 128 + g * 32), yb = *lp<f32x4>(M_YS + w * 128 + g * 32 + 16);
    const f32x4 a0 = (R < 16 ? acc[0][0] : acc[1][0]) + ya, a1 = (R < 16 ? acc[0][1] : acc[1][1]) + yb;
    __builtin_amdgcn_raw_buffer_store_b128(v4u{pk(a0[0], a0[1]), pk(a0[2], a0[3]), pk(a1[0], a1[1]), pk(a1[2], a1[3])},
                                           rOut, t == (R & 15) ? R * 1024 + g * 16 : OOB, oso, 0);
  }
  STAMP(26);
  { const u64 t0 = __builtin_amdgcn_s_memrealtime(), d = (t_b - t_a) * 8; for (int i = 0; i < 1000000 && __builtin_amdgcn_s_memrealtime() - t0 < d; ++i) {} }
}
}

extern "C" void kernel_launch(void* const* d_in, const int* in_sizes, int n_in, void* d_out, int out_size, void* d_ws, size_t ws_size, hipStream_t stream) {
  fused<<<256, 256, 0, stream>>>((const u16*)d_in[0], (const u16*)d_in[1], (const u16*)d_in[2], (const u16*)d_in[4], (const u16*)d_in[5],
                                 (const u16*)d_in[6], (const u16*)d_in[7], (u16*)d_out, (char*)d_ws);
}
```

```cpp
#include <hip/hip_runtime.h>

namespace {
typedef unsigned short u16;
typedef unsigned long long u64;
typedef unsigned v4u __attribute__((ext_vector_type(4)));
typedef unsigned v2u __attribute__((ext_vector_type(2)));
typedef float f32x4 __attribute__((ext_vector_type(4)));
typedef float f32x2 __attribute__((ext_vector_type(2)));
typedef __bf16 bf16x8 __attribute__((ext_vector_type(8)));
typedef __bf16 bf16x2 __attribute__((ext_vector_type(2)));
typedef short s16x4 __attribute__((ext_vector_type(4)));
typedef __amdgpu_buffer_rsrc_t rsrc_t;
#define LAS __attribute__((address_space(3)))
#define DEV __device__ __forceinline__

#define MAPPING 0

constexpr int W_EPOCH = 0, W_FLAGS = 4096, W_KEY = 16384, W_ST = 32768, W_PST = 65536, W_Y = 393216;
constexpr int KEY_L = 8192, ST_L = 16384, PST_L = 131072, Y_L = 98304;
constexpr int W_O = 1 << 20, W_X = 5 << 20, BIG_L = 2 << 20, W_END = 9 << 20;
constexpr int W_DBG = 16 << 20;
constexpr unsigned OOB = 0x7ff00000u;
constexpr unsigned POISON = 0xFFFFFFFFu;
constexpr int NFF = 43;
constexpr int SPIN = 1 << 15;
constexpr float EPS = 1e-5f;
constexpr int XIMG = 0, RING = 81920, MISC = 155648, LDS_BYTES = 163840;
constexpr int M_STM = MISC, M_STA = MISC + 320, M_RED = MISC + 1024, M_PS = MISC + 3072, M_TOP = MISC + 3328, M_HF = MISC + 3584,
              M_FIX = MISC + 4096, M_FST = MISC + 5120, M_YS = MISC + 5376, M_DBG = MISC + 6144, M_CS = MISC + 7168;

__shared__ __attribute__((aligned(16))) char smem[LDS_BYTES];
template <class T> DEV LAS T* lp(unsigned off) { return (LAS T*)((LAS char*)smem + off); }

#define MEMF() asm volatile("" ::: "memory")
#define WAITV(n) asm volatile("s_waitcnt vmcnt(" #n ")" ::: "memory")
#define WAITL() asm volatile("s_waitcnt lgkmcnt(0)" ::: "memory")
#define DRAIN() WAITV(0)
#define BAR() do { WAITL(); __builtin_amdgcn_s_barrier(); MEMF(); } while (0)
#define SB() __builtin_amdgcn_sched_barrier(0)
template <int N> DEV void waitv() { asm volatile("s_waitcnt vmcnt(%0)" ::"n"(N) : "memory"); }

#define STAMP(i)

#define STAMPI(i)
DEV v4u mk_srd(const void* p, unsigned bytes) { const u64 a = (u64)p; return v4u{(unsigned)a, (unsigned)(a >> 32) & 0xffffu, bytes, 0x00020000u}; }
DEV rsrc_t mk_rs(const void* p, unsigned bytes) { return __builtin_amdgcn_make_buffer_rsrc((void*)p, 0, bytes, 0x00020000); }
template <int LI, int SI> DEV void dma(v4u srd, unsigned vo, unsigned lb, unsigned sb) {
  unsigned t;
  asm volatile("s_add_i32 m0, %3, %5\n\ts_add_i32 %0, %4, %6\n\tbuffer_load_dwordx4 %1, %2, %0 offen lds"
               : "=&s"(t) : "v"(vo), "s"(srd), "s"(lb), "s"(sb), "n"(LI), "n"(SI) : "memory", "scc");
}
DEV void dma4_sc1(v4u srd, unsigned vo, unsigned lb, unsigned sb) {
  asm volatile("s_mov_b32 m0, %2\n\ts_nop 0\n\tbuffer_load_dwordx4 %0, %1, %3 offen sc1 lds\n\tbuffer_load_dwordx4 %0, %1, %3 offen offset:1024 sc1 lds\n\t"
               "buffer_load_dwordx4 %0, %1, %3 offen offset:2048 sc1 lds\n\tbuffer_load_dwordx4 %0, %1, %3 offen offset:3072 sc1 lds"
               ::"v"(vo), "s"(srd), "s"(lb), "s"(sb) : "memory");
}
DEV v4u ldc4(rsrc_t r, unsigned vo, unsigned so = 0) { return __builtin_amdgcn_raw_buffer_load_b128(r, vo, so, 16); }
DEV v2u ldc2(rsrc_t r, unsigned vo, unsigned so = 0) { return __builtin_amdgcn_raw_buffer_load_b64(r, vo, so, 16); }
DEV unsigned ldc1(rsrc_t r, unsigned vo, unsigned so = 0) { return __builtin_amdgcn_raw_buffer_load_b32(r, vo, so, 16); }
DEV void stc4(rsrc_t r, unsigned vo, v4u v, unsigned so = 0) { __builtin_amdgcn_raw_buffer_store_b128(v, r, vo, so, 16); }
DEV void stc2(rsrc_t r, unsigned vo, v2u v, unsigned so = 0) { __builtin_amdgcn_raw_buffer_store_b64(v, r, vo, so, 16); }
DEV v4u ldp4(rsrc_t r, unsigned vo, unsigned so = 0) { return __builtin_amdgcn_raw_buffer_load_b128(r, vo, so, 0); }

#define MF_(op, pre, d, a, b, ca, cb) asm volatile(pre op " %0, %1, %2, %0" : "+v"(d) : ca(a), cb(b))
#define MFZ_(op, pre, d, a, b, ca, cb) asm volatile(pre op " %0, %1, %2, 0" : "=&v"(d) : ca(a), cb(b))
#define MFC_(op, pre, d, a, b, c) asm volatile(pre op " %0, %1, %2, %3" : "=&v"(d) : "v"(a), "v"(b), "v"(c))
#define M32 "v_mfma_f32_16x16x32_bf16"
#define M16 "v_mfma_f32_16x16x16_bf16"
#define NOP2 "s_nop 1\n\t"
constexpr int AW(int s, int p) { return (s * 3 + p) * 4; }
constexpr int AO(int j, int ct) { return 192 + (j * 2 + ct) * 4; }
template <bool Z, int A> DEV void mf_av(f32x4& d, const v4u& b) {
  if constexpr (Z) asm volatile(M32 " %0, a[%c2:%c3], %1, 0" : "=&v"(d) : "v"(b), "n"(A), "n"(A + 3));
  else asm volatile(M32 " %0, a[%c2:%c3], %1, %0" : "+v"(d) : "v"(b), "n"(A), "n"(A + 3));
}
template <bool Z, int A> DEV void mf_va(f32x4& d, const v4u& b) {
  if constexpr (Z) asm volatile(M32 " %0, %1, a[%c2:%c3], 0" : "=&v"(d) : "v"(b), "n"(A), "n"(A + 3));
  else asm volatile(M32 " %0, %1, a[%c2:%c3], %0" : "+v"(d) : "v"(b), "n"(A), "n"(A + 3));
}
template <int A, int O0, int O1> DEV void trd_a(unsigned base) {
  static_assert(O0 < 65536 && O1 < 65536, "DS offset");
  asm volatile("ds_read_b64_tr_b16 a[%c1:%c2], %0 offset:%c5\n\tds_read_b64_tr_b16 a[%c3:%c4], %0 offset:%c6"
               ::"v"(base), "n"(A), "n"(A + 1), "n"(A + 2), "n"(A + 3), "n"(O0), "n"(O1) : "memory");
}
#define A8(n) "a" #n "0", "a" #n "1", "a" #n "2", "a" #n "3", "a" #n "4", "a" #n "5", "a" #n "6", "a" #n "7", "a" #n "8", "a" #n "9"
#define CLAIM_AGPRS() asm volatile("" ::: A8(), A8(1), A8(2), A8(3), A8(4), A8(5), A8(6), A8(7), A8(8), A8(9), A8(10), A8(11), A8(12), A8(13), A8(14), A8(15), A8(16), \
    A8(17), A8(18), A8(19), A8(20), A8(21), A8(22), A8(23), A8(24), "a250", "a251", "a252", "a253", "a254", "a255")
template <bool Z> DEV void mf_vv(f32x4& d, const v4u& a, const v4u& b) { if constexpr (Z) MFZ_(M32, "", d, a, b, "v", "v"); else MF_(M32, "", d, a, b, "v", "v"); }
template <bool Z> DEV void mf_vvn(f32x4& d, const v4u& a, const v4u& b) { if constexpr (Z) MFZ_(M32, NOP2, d, a, b, "v", "v"); else MF_(M32, NOP2, d, a, b, "v", "v"); }
#define TIE1(n, a) asm volatile("s_nop " #n : "+v"(a))
#define TIE2(n, a, b) asm volatile("s_nop " #n : "+v"(a), "+v"(b))
#define TIE3(n, a, b, c) asm volatile("s_nop " #n : "+v"(a), "+v"(b), "+v"(c))
#define TIE4(n, a, b, c, d) asm volatile("s_nop " #n : "+v"(a), "+v"(b), "+v"(c), "+v"(d))
#define TIE0_2(a, b) asm volatile("" : "+v"(a), "+v"(b))
#define TIE0_4(a, b, c, d) asm volatile("" : "+v"(a), "+v"(b), "+v"(c), "+v"(d))
DEV v2u trd(unsigned off) { return __builtin_bit_cast(v2u, __builtin_amdgcn_ds_read_tr16_b64_v4i16(lp<s16x4>(off))); }
DEV v4u trd2(unsigned o0, unsigned o1) { const v2u a = trd(o0), b = trd(o1); return v4u{a.x, a.y, b.x, b.y}; }
DEV unsigned pk(float a, float b) { return __builtin_bit_cast(unsigned, __builtin_convertvector((f32x2{a, b}), bf16x2)); }
DEV float blo(unsigned v) { return __builtin_bit_cast(float, v << 16); }
DEV float bhi(unsigned v) { return __builtin_bit_cast(float, v & 0xffff0000u); }
DEV float dot2(unsigned a, unsigned b, float c) { return __builtin_amdgcn_fdot2_f32_bf16(__builtin_bit_cast(bf16x2, a), __builtin_bit_cast(bf16x2, b), c, false); }
DEV float f_(unsigned v) { return __builtin_bit_cast(float, v); }
DEV unsigned u_(float v) { return __builtin_bit_cast(unsigned, v); }
template <int C> DEV unsigned dppu(unsigned v) { return (unsigned)__builtin_amdgcn_update_dpp(0, (int)v, C, 0xF, 0xF, true); }
template <int C> DEV float dppf(float v) { return f_(dppu<C>(u_(v))); }
DEV float rowsum(float v) { v += dppf<0x128>(v); v += dppf<0x124>(v); v += dppf<0x122>(v); v += dppf<0x121>(v); return v; }
DEV unsigned rowmaxu(unsigned v) { v = max(v, dppu<0x128>(v)); v = max(v, dppu<0x124>(v)); v = max(v, dppu<0x122>(v)); v = max(v, dppu<0x121>(v)); return v; }
DEV float x16sum(float v) { auto r = __builtin_amdgcn_permlane16_swap(u_(v), u_(v), false, false); return f_(r[0]) + f_(r[1]); }
DEV float x32sum(float v) { auto r = __builtin_amdgcn_permlane32_swap(u_(v), u_(v), false, false); return f_(r[0]) + f_(r[1]); }
DEV float x16max(float v) { auto r = __builtin_amdgcn_permlane16_swap(u_(v), u_(v), false, false); return fmaxf(f_(r[0]), f_(r[1])); }
DEV float x32max(float v) { auto r = __builtin_amdgcn_permlane32_swap(u_(v), u_(v), false, false); return fmaxf(f_(r[0]), f_(r[1])); }
DEV unsigned x16maxu(unsigned v) { auto r = __builtin_amdgcn_permlane16_swap(v, v, false, false); return max((unsigned)r[0], (unsigned)r[1]); }
DEV unsigned x32maxu(unsigned v) { auto r = __builtin_amdgcn_permlane32_swap(v, v, false, false); return max((unsigned)r[0], (unsigned)r[1]); }
DEV unsigned rfl(unsigned v) { return (unsigned)__builtin_amdgcn_readfirstlane((int)v); }
DEV float rsq(float v) { return __builtin_amdgcn_rsqf(v); }
DEV float rcp(float v) { return __builtin_amdgcn_rcpf(v); }
DEV float ex2(float v) { return __builtin_amdgcn_exp2f(v); }
DEV bool isnan_(float v) { return v != v; }
constexpr unsigned ONE2 = 0x3F803F80u;
constexpr float LOG2E = 1.4426950408889634f;

template <int S> struct IC { static constexpr int v = S; };
template <int A, int B, class F> DEV void sfor(F&& f) { if constexpr (A < B) { f(IC<A>{}); sfor<A + 1, B>(f); } }

struct Ctx {
  int lane, w, t, g, c, hg, c2, u, q, ff;
  rsrc_t rS;
  DEV void fresh() { asm volatile("" : "+v"(lane)); t = lane & 15; g = lane >> 4; }
};

struct Acc { f32x4 Q[5], K[5], V[5]; };

DEV void attention(Ctx& x, const Acc& a, int L) {
  x.fresh();
  const int t = x.t, g = x.g;
  const f32x4 csq = *lp<f32x4>(M_CS + x.w * 192 + g * 16), csk = *lp<f32x4>(M_CS + x.w * 192 + 64 + g * 16);
  const float csv = *lp<float>(M_CS + x.w * 192 + 128 + t * 4);
  v2u Qp[5], Kp[5], Vp[5];
  float cq[5];
#pragma unroll
  for (int i = 0; i < 5; ++i) {
    const float m = *lp<float>(M_STM + (16 * i + t) * 4), s = *lp<float>(M_STA + (16 * i + t) * 4);
    const f32x4 m4 = *lp<f32x4>(M_STM + (16 * i + 4 * g) * 4), s4 = *lp<f32x4>(M_STA + (16 * i + 4 * g) * 4);
    const f32x4 k = (a.K[i] - m * csk) * s;
    Kp[i] = v2u{pk(k[0], k[1]), pk(k[2], k[3])};
    const f32x4 v = (a.V[i] - m4 * csv) * s4;
    Vp[i] = v2u{pk(v[0], v[1]), pk(v[2], v[3])};
    if (i > 0) { const f32x4 qq = a.Q[i] - m * csq; Qp[i] = v2u{pk(qq[0], qq[1]), pk(qq[2], qq[3])}; cq[i] = s * (0.25f * LOG2E); }
  }
  f32x4 bp, bc;
#pragma unroll
  for (int r = 0; r < 4; ++r) {
    const int kk = 4 * g + r; const bool par = ((t ^ r) & 1) == 0;
    bc[r] = (par && kk <= t) ? 0.f : -1e30f;
    bp[r] = (par && kk >= t + 2) ? 0.f : -1e30f;
  }
  const v4u ones = {ONE2, ONE2, ONE2, ONE2};
  f32x4 b0 = bp;
  if (x.c == 0) b0 = f32x4{-1e30f, -1e30f, -1e30f, -1e30f};
  f32x4 sp[5], sc[5], o[5], l[5];
#pragma unroll
  for (int i = 1; i < 5; ++i) {
    if (i == 1) MFC_(M16, NOP2, sp[i], Kp[i - 1], Qp[i], b0); else MFC_(M16, NOP2, sp[i], Kp[i - 1], Qp[i], bp);
    MFC_(M16, NOP2, sc[i], Kp[i], Qp[i], bc);
  }
  v2u Op[5];
#pragma unroll
  for (int i = 1; i < 5; ++i) {
    TIE0_2(sp[i], sc[i]);
    float mx = fmaxf(fmaxf(fmaxf(sp[i][0], sp[i][1]), fmaxf(sp[i][2], sp[i][3])), fmaxf(fmaxf(sc[i][0], sc[i][1]), fmaxf(sc[i][2], sc[i][3])));
    mx = x32max(x16max(mx));
    const float cm = -cq[i] * mx;
    const f32x4 ep = sp[i] * cq[i] + cm, ec = sc[i] * cq[i] + cm;
    const v4u P = {pk(ex2(ep[0]), ex2(ep[1])), pk(ex2(ep[2]), ex2(ep[3])), pk(ex2(ec[0]), ex2(ec[1])), pk(ex2(ec[2]), ex2(ec[3]))};
    const v4u VV = {Vp[i - 1].x, Vp[i - 1].y, Vp[i].x, Vp[i].y};
    mf_vvn<true>(l[i], ones, P);
    mf_vvn<true>(o[i], VV, P);
    if (i > 1) {
      TIE0_2(o[i - 1], l[i - 1]);
      const f32x4 on = o[i - 1] * rcp(l[i - 1][0]);
      Op[i - 1] = v2u{pk(on[0], on[1]), pk(on[2], on[3])};
    }
  }
  TIE2(7, o[4], l[4]);
  { const f32x4 on = o[4] * rcp(l[4][0]); Op[4] = v2u{pk(on[0], on[1]), pk(on[2], on[3])}; }
  const unsigned ob = W_O + L * BIG_L + (x.c * 64 + 4 * x.hg + x.w) * 1024;
  stc4(x.rS, x.lane * 16, v4u{Op[1].x, Op[1].y, Op[2].x, Op[2].y}, ob);
  stc4(x.rS, x.lane * 16, v4u{Op[3].x, Op[3].y, Op[4].x, Op[4].y}, ob + 32 * 1024);
}

DEV void top2(const Ctx& x, int L, unsigned& k1, unsigned& k2, bool nap) {
  if (x.w == 0) {
    v2u k;
    for (int sp = 0; sp < SPIN; ++sp) {
      MEMF();
      k = ldc2(x.rS, x.lane * 128, W_KEY + L * KEY_L);
      if (__all(k.x != POISON && k.y != POISON)) break;
      if (nap) __builtin_amdgcn_s_sleep(4);
    }
    const unsigned m1 = x32maxu(x16maxu(rowmaxu(k.x)));
    const unsigned m2 = x32maxu(x16maxu(rowmaxu(k.x == m1 ? k.y : k.x)));
    *lp<v2u>(M_TOP) = v2u{m1, m2};
  }
  BAR();
  const v2u k = *lp<v2u>(M_TOP);
  k1 = rfl(k.x); k2 = rfl(k.y);
}
DEV int key_tok(unsigned k) { return 2047 - (int)(k & 0x7FFu); }
DEV float key_sig(unsigned k) {
  unsigned u = (k & ~0x7FFu) | 0x400u;
  u = (u & 0x80000000u) ? (u ^ 0x80000000u) : ~u;
  return rcp(1.f + ex2(-LOG2E * f_(u)));
}

struct Ffn { v4u GU[4][4]; v4u WD[8]; float cs[4]; };
DEV void ffn_load_down(const Ctx& x, rsrc_t rD, v4u (&raw)[8]) {
#pragma unroll
  for (int e = 0; e < 8; ++e) raw[e] = ldp4(rD, (8 * x.g + e) * 1024 + x.t * 16, x.ff * 32768 + x.w * 256);
}
DEV void ffn_prep(const Ctx& x, const v4u (&raw)[8], Ffn& F) {
  const int qq = x.t >> 2, p = x.lane & 3;
  const unsigned b0 = RING + (128 * x.w + 8 * x.g + qq) * 64 + (x.g & 1) * 32 + 8 * p, b1 = b0 ^ 32;
  const v4u ones = {ONE2, ONE2, ONE2, ONE2};
#pragma unroll
  for (int c = 0; c < 8; ++c)
#pragma unroll
    for (int d = 0; d < 4; ++d) F.WD[c][d] = __builtin_amdgcn_perm(raw[2 * d + 1][c >> 1], raw[2 * d][c >> 1], (c & 1) ? 0x07060302u : 0x05040100u);
  TIE0_4(F.WD[0], F.WD[1], F.WD[2], F.WD[3]); TIE0_4(F.WD[4], F.WD[5], F.WD[6], F.WD[7]);
  SB();
  f32x4 cs[4];
#pragma unroll
  for (int s = 0; s < 4; ++s)
#pragma unroll
    for (int m = 0; m < 4; ++m) {
      const unsigned o = ((m & 1) ? b1 : b0) + s * 2048 + (m >> 1) * 32768;
      F.GU[s][m] = trd2(o, o + 256);
    }
#pragma unroll
  for (int s = 0; s < 4; ++s)
#pragma unroll
    for (int m = 0; m < 4; ++m) { if (s == 0) mf_vvn<true>(cs[m], ones, F.GU[s][m]); else mf_vv<false>(cs[m], ones, F.GU[s][m]); }
  TIE4(7, cs[0], cs[1], cs[2], cs[3]);
#pragma unroll
  for (int m = 0; m < 4; ++m) F.cs[m] = cs[m][0];
}
DEV void ffn_run(const Ctx& x, const Ffn& F, int L, unsigned k1, unsigned k2) {
  const int ta = key_tok(k1), tb = key_tok(k2);
  const int tk = ((x.t >> 2) & 1) ? tb : ta;
  const unsigned xo = ((tk >> 4) * 16 + 4 * x.w) * 1024 + (16 * x.g + (tk & 15)) * 16;
  v4u xf[4]; v2u sa, sb;
  for (int sp = 0; sp < SPIN; ++sp) {
    MEMF();
#pragma unroll
    for (int s = 0; s < 4; ++s) xf[s] = ldc4(x.rS, xo + s * 1024, W_X + L * BIG_L);
    sa = ldc2(x.rS, ta * 8, W_ST + L * ST_L); sb = ldc2(x.rS, tb * 8, W_ST + L * ST_L);
    const unsigned m = max(max(max(xf[0].x, xf[1].x), max(xf[2].x, xf[3].x)), max(sa.x, sb.x));
    if (__all(m != POISON)) break;
  }
  f32x4 d[4];
#pragma unroll
  for (int s = 0; s < 4; ++s)
#pragma unroll
    for (int m = 0; m < 4; ++m) { if (s == 0) mf_vvn<true>(d[m], xf[s], F.GU[s][m]); else mf_vv<false>(d[m], xf[s], F.GU[s][m]); }
  TIE4(7, d[0], d[1], d[2], d[3]);
  const float mt = (x.g & 1) ? f_(sb.x) : f_(sa.x);
  const bool hi = x.g >= 2;
  const float vg = (hi ? d[1][0] : d[0][0]) - mt * (hi ? F.cs[1] : F.cs[0]);
  const float vu = (hi ? d[3][0] : d[2][0]) - mt * (hi ? F.cs[3] : F.cs[2]);
  const unsigned ro = M_RED + ((x.g & 1) * 32 + 16 * (x.g >> 1) + x.t) * 16 + 4 * x.w;
  *lp<float>(ro) = vg; *lp<float>(ro + 1024) = vu;
  BAR();
  const f32x4 g4 = *lp<f32x4>(M_RED + x.lane * 16), u4 = *lp<f32x4>(M_RED + 1024 + x.lane * 16);
  const float rs = rsq((hi ? f_(sb.y) : f_(sa.y)) + EPS), sg = hi ? key_sig(k2) : key_sig(k1);
  const float gv = ((g4[0] + g4[1]) + (g4[2] + g4[3])) * rs, uv = ((u4[0] + u4[1]) + (u4[2] + u4[3])) * rs;
  const float h = gv * rcp(1.f + ex2(-LOG2E * gv)) * uv * sg;
  *lp<u16>(M_HF + x.w * 128 + x.lane * 2) = (u16)pk(h, h);
  const v4u hf = *lp<v4u>(M_HF + x.w * 128 + ((x.t >> 2) & 1) * 64 + x.g * 16);
  f32x4 y4[8];
#pragma unroll
  for (int c = 0; c < 8; ++c) mf_vv<true>(y4[c], hf, F.WD[c]);
  TIE4(3, y4[0], y4[1], y4[2], y4[3]); TIE4(3, y4[4], y4[5], y4[6], y4[7]);
  float y[8];
#pragma unroll
  for (int c = 0; c < 8; ++c) y[c] = y4[c][0];
  const unsigned yo = hi ? OOB : ((x.g & 1) * 44 + x.ff) * 1024 + x.w * 256 + x.t * 16;
  stc4(x.rS, yo, v4u{pk(y[0], y[1]), pk(y[2], y[3]), pk(y[4], y[5]), pk(y[6], y[7])}, W_Y + L * Y_L);
}
DEV v4u sel_frag(int t) {
  const unsigned v = (t & 1) ? 0x3F800000u : 0x00003F80u;
  return v4u{(t >> 1) == 0 ? v : 0u, (t >> 1) == 1 ? v : 0u, (t >> 1) == 2 ? v : 0u, (t >> 1) == 3 ? v : 0u};
}
DEV void y_slice(const Ctx& x, int L, int slot) {
  const v4u sel = sel_frag(x.t);
  const int p0 = 4 * (x.t >> 2) + x.g;
  const unsigned o = (slot * 44 + p0) * 1024 + (128 * x.q + 32 * x.w) * 2 + (x.t & 3) * 16;
  f32x4 d;
  for (int sp = 0; sp < SPIN; ++sp) {
    MEMF();
    const v4u a = ldc4(x.rS, o, W_Y + L * Y_L), b = ldc4(x.rS, o + 16384, W_Y + L * Y_L), c = ldc4(x.rS, p0 < NFF - 32 ? o + 32768 : OOB, W_Y + L * Y_L);
    mf_vvn<true>(d, sel, a); mf_vvn<false>(d, sel, b); mf_vvn<false>(d, sel, c);
    TIE1(7, d);
    if (__all(max(max(a.x, b.x), c.x) != POISON)) break;
  }
#pragma unroll
  for (int r = 0; r < 4; ++r) { d[r] += dppf<0x124>(d[r]); d[r] += dppf<0x128>(d[r]); }
  if (x.g < 2) *lp<f32x4>(M_YS + x.w * 128 + ((x.t & 3) * 8 + 4 * x.g) * 4) = d;
}

DEV void outproj(Ctx& x, const v4u (&WO)[8][2], const v4u (&ob)[8], f32x4 (&acc)[2][2], const v4u wfr, const float wsum, int L, v4u (&xk)[2]) {
  const int w = x.w;
#pragma unroll
  for (int j = 0; j < 4; ++j) {
    const v4u a = ob[2 * j], b = ob[2 * j + 1];
    *lp<v4u>(XIMG + (4 * w + j) * 1024 + x.lane * 16) = v4u{a.x, a.y, b.x, b.y};
    *lp<v4u>(XIMG + (16 + 4 * w + j) * 1024 + x.lane * 16) = v4u{a.z, a.w, b.z, b.w};
  }
  BAR();
  STAMPI(7 + 20 * L);
  {
    v4u of[3][2];
#pragma unroll
    for (int j = 0; j < 2; ++j) { of[j][0] = *lp<v4u>(XIMG + j * 1024 + x.lane * 16); of[j][1] = *lp<v4u>(XIMG + (16 + j) * 1024 + x.lane * 16); }
    TIE4(1, acc[0][0], acc[0][1], acc[1][0], acc[1][1]);
    sfor<0, 32>([&](auto J) {
      constexpr int j = J.v >> 1, T = J.v & 1;
      if constexpr (j < 8) mf_av<false, AO(j, 0)>(acc[T][0], of[j % 3][T]); else mf_vv<false>(acc[T][0], WO[j - 8][0], of[j % 3][T]);
      if constexpr (j + 2 < 16) of[(j + 2) % 3][T] = *lp<v4u>(XIMG + (16 * T + j + 2) * 1024 + x.lane * 16);
      SB();
      if constexpr (j < 8) mf_av<false, AO(j, 1)>(acc[T][1], of[j % 3][T]); else mf_vv<false>(acc[T][1], WO[j - 8][1], of[j % 3][T]);
      SB();
    });
    TIE4(7, acc[0][0], acc[0][1], acc[1][0], acc[1][1]);
  }
  STAMPI(8 + 12 * L);
#pragma unroll
  for (int T = 0; T < 2; ++T) {
    xk[T] = v4u{pk(acc[T][0][0], acc[T][0][1]), pk(acc[T][0][2], acc[T][0][3]), pk(acc[T][1][0], acc[T][1][1]), pk(acc[T][1][2], acc[T][1][3])};
    stc4(x.rS, x.lane * 16, xk[T], W_X + L * BIG_L + ((4 * x.c2 + 2 * x.u + T) * 16 + 4 * x.q + w) * 1024);
  }
  f32x4 dd[2];
  mf_vvn<true>(dd[0], wfr, xk[0]); mf_vvn<true>(dd[1], wfr, xk[1]);
  TIE2(7, dd[0], dd[1]);
#pragma unroll
  for (int T = 0; T < 2; ++T) {
    const f32x4 d = dd[T];
    float s2 = dot2(xk[T].x, xk[T].x, dot2(xk[T].y, xk[T].y, 0.f)) + dot2(xk[T].z, xk[T].z, dot2(xk[T].w, xk[T].w, 0.f));
    s2 = x32sum(x16sum(s2));
    if (x.g == 0) *lp<f32x4>(M_RED + (w * 32 + 16 * T + x.t) * 16) = f32x4{d[0], d[1], s2, 0.f};
  }
  BAR();
  if (w == 0) {
    const int tk = x.lane & 31, hf = x.lane >> 5;
    const unsigned pb = W_PST + L * PST_L + (x.c2 * 2 + x.u) * 2048;
    {
      const f32x4 a = *lp<f32x4>(M_RED + (hf * 64 + tk) * 16) + *lp<f32x4>(M_RED + (hf * 64 + 32 + tk) * 16);
      const f32x4 s = {x32sum(a[0]), x32sum(a[1]), x32sum(a[2]), 0.f};
      stc4(x.rS, hf ? OOB : x.q * 512 + tk * 16, __builtin_bit_cast(v4u, s), pb);
    }
    STAMPI(57 + 3 * L);
    DRAIN();
    STAMPI(58 + 3 * L);
    v4u a, b;
    for (int sp = 0; sp < SPIN; ++sp) {
      MEMF();
      a = ldc4(x.rS, hf * 1024 + tk * 16, pb); b = ldc4(x.rS, hf * 1024 + 512 + tk * 16, pb);
      if (__all(a.w != POISON && b.w != POISON)) break;
    }
    STAMPI(59 + 3 * L);
    const float s1 = x32sum(f_(a.x) + f_(b.x)), sw = x32sum(f_(a.y) + f_(b.y)), s2 = x32sum(f_(a.z) + f_(b.z));
    const float m = s1 * (1.f / 512), var = fmaxf(s2 * (1.f / 512) - m * m, 0.f), rs = rsq(var + EPS);
    if (x.q == 0) {
      const float r = rs * (sw - m * wsum);
      unsigned k = u_(r); k = (k & 0x80000000u) ? ~k : (k | 0x80000000u);
      const int tok = x.c2 * 64 + x.u * 32 + tk;
      k = (k & ~0x7FFu) | (unsigned)(2047 - tok);
      const unsigned m1 = x16maxu(rowmaxu(k)), m2 = x16maxu(rowmaxu(k == m1 ? 0u : k));
      stc2(x.rS, x.lane == 0 ? (x.c2 * 2 + x.u) * 128 : OOB, v2u{m1, m2}, W_KEY + L * KEY_L);
      stc2(x.rS, hf ? OOB : tok * 8, v2u{u_(m), u_(var)}, W_ST + L * ST_L);
    }
    *lp<f32x2>(M_PS + tk * 8) = f32x2{m, rs};
  }
}
DEV void load_o(const Ctx& x, int L, v4u (&ob)[8]) {
  const unsigned so = W_O + L * BIG_L + ((x.c2 * 2 + x.u) * 32 + 8 * x.w) * 1024;
  for (int sp = 0; sp < SPIN; ++sp) {
    MEMF();
#pragma unroll
    for (int i = 0; i < 8; ++i) ob[i] = ldc4(x.rS, x.lane * 16 + i * 1024, so);
    const unsigned m = max(max(max(ob[0].x, ob[1].x), max(ob[2].x, ob[3].x)), max(max(ob[4].x, ob[5].x), max(ob[6].x, ob[7].x)));
    if (__all(m != POISON)) break;
  }
}
DEV void unpack8(v4u v, f32x4& a, f32x4& b) { a = f32x4{blo(v.x), bhi(v.x), blo(v.y), bhi(v.y)}; b = f32x4{blo(v.z), bhi(v.z), blo(v.w), bhi(v.w)}; }

__global__ void __launch_bounds__(256, 1)
fused(const u16* __restrict__ x0, const u16* __restrict__ wqkv, const u16* __restrict__ wo, const u16* __restrict__ wr, const u16* __restrict__ wg,
      const u16* __restrict__ wu, const u16* __restrict__ wd, u16* __restrict__ out, char* __restrict__ ws) {
  Ctx x;
  const int b = blockIdx.x, tid = threadIdx.x, lane = tid & 63, w = __builtin_amdgcn_readfirstlane(tid >> 6), t = lane & 15, g = lane >> 4;
  const int xc = b & 7, jb = b >> 3;
  x.lane = lane; x.w = w; x.t = t; x.g = g;
  x.c = xc * 4 + (jb >> 3); x.hg = jb & 7; x.c2 = x.c; x.u = x.hg >> 2; x.q = x.hg & 3;
  { const int f = (jb / 6) * 8 + xc; x.ff = (jb % 6 == 1 && f < NFF) ? f : -1; }
  const int c = x.c, hg = x.hg;
  x.rS = mk_rs(ws, W_END);
  const v4u sS = mk_srd(ws, W_END), sX = mk_srd(x0, 2048 * 1024), sW = mk_srd(wqkv, 512 * 3072), sO = mk_srd(wo, 512 * 1024);
  CLAIM_AGPRS();
  STAMP(0);

  v2u ep;
  asm volatile("buffer_load_dwordx2 %0, off, %1, 0 sc1" : "=v"(ep) : "s"(sS) : "memory");
  sfor<0, 20>([&](auto I) {
    constexpr int i = I.v;
    const int R = 20 * w + i, tok = (c == 0 && R < 16) ? R : 64 * c - 16 + R;
    dma<i * 1024, 0>(sX, (lane ^ (R & 15)) * 16, XIMG + 20 * w * 1024, tok * 1024);
  });
  const unsigned wvo = (8 * w + (lane >> 3)) * 3072 + hg * 128 + (((((lane & 7) >> 1) ^ (((lane >> 4) & 1) + 2 * (w & 1))) * 2) + (lane & 1)) * 16;
  const unsigned ringw = RING + w * 1024;
  auto issue_w = [&](auto S) {
    constexpr int s = S.v;
    sfor<0, 3>([&](auto P) { dma<(s % 6) * 12288 + P.v * 4096, s * 98304 + P.v * 1024>(sW, wvo, ringw, 0); });
  };
  const unsigned ovo = g * 1024 + x.q * 256 + (t ^ (4 * g)) * 16;
  auto issue_o_ring = [&](auto S) {
    constexpr int sl = S.v;
    sfor<0, 3>([&](auto P) { dma<(12 * sl + P.v) * 1024, (12 * sl + P.v) * 4096>(sO, ovo, RING + 3 * w * 1024, 3 * w * 4096); });
  };
  const v4u pz = {POISON, POISON, POISON, POISON};
  sfor<0, 5>([&](auto I) {
    constexpr int i = I.v;
    waitv<16 + i>();
    STAMP(50 + i);
    if constexpr (i < 2) {
      const unsigned ob = W_O + i * BIG_L + (c * 64 + 4 * hg + w) * 1024;
      stc4(x.rS, lane * 16, pz, ob); stc4(x.rS, lane * 16, pz, ob + 32768);
    } else if constexpr (i < 4) {
      const unsigned xb = W_X + (i - 2) * BIG_L + ((4 * x.c2 + 2 * x.u) * 16 + 4 * x.q + w) * 1024;
      stc4(x.rS, lane * 16, pz, xb); stc4(x.rS, lane * 16, pz, xb + 16384);
    } else {
      const int gi = x.c2 * 2 + x.u;
      unsigned so = OOB;
      if (w == 1) so = W_PST + (lane >> 5) * PST_L + gi * 2048 + x.q * 512 + (lane & 31) * 16;
      if (w == 2 && x.q == 0) so = lane < 32 ? W_ST + g * ST_L + gi * 256 + t * 16 : (lane < 34 ? W_KEY + (lane - 32) * KEY_L + gi * 128 : OOB);
      stc4(x.rS, so, pz);
      stc4(x.rS, x.ff >= 0 ? W_Y + (w >> 1) * Y_L + ((w & 1) * 44 + x.ff) * 1024 + lane * 16 : OOB, pz);
    }
    issue_w(I);
    const int R = 20 * w + 4 * i + g;
    const unsigned ra = XIMG + R * 1024 + t * 16;
    float s1a = 0.f, s1b = 0.f, s2a = 0.f, s2b = 0.f;
#pragma unroll
    for (int k = 0; k < 4; ++k) {
      const v4u v = *lp<v4u>(ra + k * 256);
      s1a = dot2(v.x, ONE2, s1a); s2a = dot2(v.x, v.x, s2a); s1b = dot2(v.y, ONE2, s1b); s2b = dot2(v.y, v.y, s2b);
      s1a = dot2(v.z, ONE2, s1a); s2a = dot2(v.z, v.z, s2a); s1b = dot2(v.w, ONE2, s1b); s2b = dot2(v.w, v.w, s2b);
    }
    const float m = rowsum(s1a + s1b) * (1.f / 512), var = fmaxf(rowsum(s2a + s2b) * (1.f / 512) - m * m, 0.f);
    *lp<float>(M_STM + R * 4) = m; *lp<float>(M_STA + R * 4) = rsq(var + EPS);
    if (i == 0 && w == 0) *lp<v4u>(XIMG + lane * 16) = v4u{ONE2, ONE2, ONE2, ONE2};
    SB();
  });
  asm volatile("" : "+v"(ep));
  const unsigned e0 = rfl(ep.x) + 1u, e1 = rfl(ep.y);
  STAMP(1);

  Acc a;
  {
    unsigned xb[4];
#pragma unroll
    for (int k = 0; k < 4; ++k) xb[k] = XIMG + t * 1024 + (((4 * k) ^ (t & 12)) | (g ^ (t & 3))) * 16;
    const int qq = t >> 2;
    const unsigned wb = RING + (8 * g + qq) * 128 + (w ^ ((qq >> 1) + 2 * (g & 1))) * 32 + 8 * (lane & 3);
    const unsigned wb2 = wb + 36864;
    v4u xf[2][5];
    sfor<0, 17>([&](auto S) {
      constexpr int s = S.v;
      if constexpr (s < 16) {
        waitv<(s < 4 ? 20 - 2 * s : 12)>();
        BAR();
        STAMP(32 + s);
      }
      auto mm = [&](auto N) {
        constexpr int n = N.v;
        if constexpr (s > 0) {
          constexpr int i = n / 3, p = n % 3, z = (s - 1) & 1;
          if constexpr (p == 0) mf_av<s == 1, AW(s - 1, 1)>(a.K[i], xf[z][i]);
          if constexpr (p == 1) mf_va<s == 1, AW(s - 1, 2)>(a.V[i], xf[z][i]);
          if constexpr (p == 2) mf_av<s == 1, AW(s - 1, 0)>(a.Q[i], xf[z][i]);
        }
        SB();
      };
      sfor<0, 15>([&](auto N) {
        constexpr int n = N.v;
        mm(N);
        if constexpr (s < 16) {
          if constexpr (n < 3) {
            if constexpr (s + 5 < 16) dma<((s + 5) % 6) * 12288 + n * 4096, (s + 5) * 98304 + n * 1024>(sW, wvo, ringw, 0);
            else dma<(12 * ((s + 5) % 6) + n) * 1024, (12 * ((s + 5) % 6) + n) * 4096>(sO, ovo, RING + 3 * w * 1024, 3 * w * 4096);
          } else if constexpr (n < 6) {
            constexpr int o = (s % 3) * 12288 + (n - 3) * 4096;
            trd_a<AW(s, n - 3), o, o + 512>((s % 6) < 3 ? wb : wb2);
          } else if constexpr (n < 11) {
            xf[s & 1][n - 6] = *lp<v4u>(xb[s & 3] + (n - 6) * 16384 + (s >> 2) * 256);
          } else if constexpr (n == 11 && s == 4) {
            stc2(x.rS, lane == 0 ? W_FLAGS + (b * 4 + w) * 8 : OOB, v2u{e0, e1});
          }
          SB();
        }
      });
      if constexpr (s == 15) {
        BAR();
      }
    });
  }
  TIE3(7, a.Q[4], a.K[4], a.V[4]);
#pragma unroll
  for (int i = 0; i < 4; ++i) asm volatile("" : "+v"(a.Q[i]), "+v"(a.K[i]), "+v"(a.V[i]));
  STAMP(2);
  const rsrc_t rX = mk_rs(x0, 2048 * 1024), rR = mk_rs(wr, 1024);
  const int n0 = 128 * x.q + 32 * w;
  v4u xk[2];
#pragma unroll
  for (int T = 0; T < 2; ++T) xk[T] = ldp4(rX, (16 * T + t) * 1024 + g * 16, (64 * x.c2 + 32 * x.u) * 1024 + n0 * 2);
  const v4u wr8 = ldp4(rR, g * 16, n0 * 2), wrall = ldp4(rR, lane * 16);
  issue_o_ring(IC<3>{});
  sfor<0, 14>([&](auto K) { dma<K.v * 1024, K.v * 4096>(sO, ovo, XIMG + 24576 + 14 * w * 1024, (72 + 14 * w) * 4096); });
  v4u fl0 = ldc4(x.rS, lane * 16, W_FLAGS + w * 2048), fl1 = ldc4(x.rS, lane * 16 + 1024, W_FLAGS + w * 2048);
  SB();
  STAMP(56);
  if (t == 0) { *lp<f32x4>(M_CS + w * 192 + g * 16) = a.Q[0]; *lp<f32x4>(M_CS + w * 192 + 64 + g * 16) = a.K[0]; }
  if (g == 0) *lp<float>(M_CS + w * 192 + 128 + t * 4) = a.V[0][0];
  attention(x, a, 0);
  STAMP(3);
  v4u WO[8][2];
  const unsigned obs = (4 * g + (t >> 2)) * 256 + ((4 * w + (lane & 3)) ^ (t & 12)) * 16;
  waitv<18>();
  BAR();
  sfor<0, 16>([&](auto J) { constexpr int j = J.v >> 1, ct = J.v & 1; trd_a<AO(j, ct), j * 8192 + 8 * ct, j * 8192 + 4096 + 8 * ct>(RING + obs); });
#pragma unroll
  for (int ct = 0; ct < 2; ++ct) WO[0][ct] = trd2(RING + 65536 + obs + 8 * ct, RING + 65536 + obs + 4096 + 8 * ct);
  SB();
  DRAIN();
  for (int sp = 0; sp < SPIN; ++sp) {
    if (__all(fl0.x == e0 && fl0.y == e1 && fl0.z == e0 && fl0.w == e1 && fl1.x == e0 && fl1.y == e1 && fl1.z == e0 && fl1.w == e1)) break;
    MEMF();
    fl0 = ldc4(x.rS, lane * 16, W_FLAGS + w * 2048); fl1 = ldc4(x.rS, lane * 16 + 1024, W_FLAGS + w * 2048);
  }
  BAR();
  STAMP(4);
  if (b == 0 && w == 3) stc2(x.rS, lane == 0 ? W_EPOCH : OOB, v2u{e0, e1});
  v4u ob[8];
  {
    const unsigned so = W_O + ((x.c2 * 2 + x.u) * 32 + 8 * w) * 1024;
#pragma unroll
    for (int i = 0; i < 8; ++i) ob[i] = ldc4(x.rS, lane * 16 + i * 1024, so);
    SB();
#pragma unroll
    for (int j = 9; j < 16; ++j)
#pragma unroll
      for (int ct = 0; ct < 2; ++ct) WO[j - 8][ct] = trd2(XIMG + 24576 + obs + (j - 9) * 8192 + 8 * ct, XIMG + 24576 + obs + (j - 9) * 8192 + 4096 + 8 * ct);
    SB();
    const unsigned m = max(max(max(ob[0].x, ob[1].x), max(ob[2].x, ob[3].x)), max(max(ob[4].x, ob[5].x), max(ob[6].x, ob[7].x)));
    if (!__all(m != POISON)) load_o(x, 0, ob);
  }
  STAMP(5);
  const v4u wfr = t == 0 ? v4u{ONE2, ONE2, ONE2, ONE2} : (t == 1 ? wr8 : v4u{0u, 0u, 0u, 0u});
  const float wsum = x32sum(x16sum(rowsum(dot2(wrall.x, ONE2, dot2(wrall.y, ONE2, 0.f)) + dot2(wrall.z, ONE2, dot2(wrall.w, ONE2, 0.f)))));
  f32x4 acc[2][2];
#pragma unroll
  for (int T = 0; T < 2; ++T) unpack8(xk[T], acc[T][0], acc[T][1]);
  BAR();
  if (x.ff >= 0) {
    const v4u sG = mk_srd(wg, 512 * 2752), sU = mk_srd(wu, 512 * 2752);
    const unsigned gvo = (lane >> 2) * 2752 + x.ff * 64 + (((((lane >> 1) & 1) ^ (lane >> 5)) * 2) + (lane & 1)) * 16;
    sfor<0, 8>([&](auto K) {
      dma<K.v * 1024, K.v * 16 * 2752>(sG, gvo, RING + w * 8192, w * 128 * 2752);
      dma<32768 + K.v * 1024, K.v * 16 * 2752>(sU, gvo, RING + w * 8192, w * 128 * 2752);
    });
  }
  outproj(x, WO, ob, acc, wfr, wsum, 0, xk);
  STAMP(9);
  const rsrc_t rD = mk_rs(wd, 1376 * 1024);
  if (w == 1) {
    DRAIN();
    const int gi = max(2 * c - 1 + (t >> 2), 0);
    const unsigned o = t < 12 ? gi * 2048 + (t & 3) * 512 + 12 : OOB;
    for (int sp = 0; sp < SPIN; ++sp) {
      MEMF();
      if (__all(ldc1(x.rS, o, W_PST) != POISON)) break;
    }
  }
  BAR();
  STAMP(10);

  unsigned k1 = 0, k2 = 0;
  auto issue_x1 = [&]() {
#pragma unroll
    for (int k = 0; k < 5; ++k) {
      const int n = 20 * w + 4 * k;
      dma4_sc1(sS, lane * 16, XIMG + n * 1024, W_X + ((4 * c - 1) * 16 + n + ((c == 0 && n < 16) ? 16 : 0)) * 1024);
    }
  };
  issue_x1();
  if (x.ff >= 0) {
    v4u raw[8]; Ffn F;
    ffn_load_down(x, rD, raw);
    DRAIN();
    ffn_prep(x, raw, F);
    top2(x, 0, k1, k2, false);
    STAMP(11);
    ffn_run(x, F, 0, k1, k2);
    STAMP(12);
  }
#pragma unroll 1
  for (int tries = 0; tries < 64; ++tries) {
    DRAIN();
    BAR();
    STAMP(28);
    v4u xf[2][5];
#pragma unroll
    for (int i = 0; i < 5; ++i) xf[0][i] = *lp<v4u>(XIMG + i * 16384 + lane * 16);
    sfor<0, 16>([&](auto S) {
      constexpr int s = S.v;
      sfor<0, 5>([&](auto I) {
        constexpr int i = I.v;
        mf_av<s == 0, AW(s, 1)>(a.K[i], xf[s & 1][i]);
        if constexpr (s < 15) xf[(s + 1) & 1][i] = *lp<v4u>(XIMG + (i * 16 + s + 1) * 1024 + lane * 16);
        SB();
        mf_va<s == 0, AW(s, 2)>(a.V[i], xf[s & 1][i]);
        if constexpr (i > 0) mf_av<s == 0, AW(s, 0)>(a.Q[i], xf[s & 1][i]);
        SB();
      });
    });
    TIE3(7, a.Q[4], a.K[4], a.V[4]);
#pragma unroll
    for (int i = 1; i < 4; ++i) asm volatile("" : "+v"(a.Q[i]), "+v"(a.K[i]), "+v"(a.V[i]));
    TIE0_2(a.K[0], a.V[0]);
    const float chk = (a.K[0][0] + a.K[1][0]) + (a.K[2][0] + a.K[3][0]) + a.K[4][0];
    if (!__any(isnan_(chk))) break;
    BAR();
    issue_x1();
  }
  STAMP(13);
  if (w == 1) {
    v2u sa, sb;
    const int ta = 64 * c - 16 + lane;
    for (int sp = 0; sp < SPIN; ++sp) {
      MEMF();
      sa = ldc2(x.rS, max(ta, 0) * 8, W_ST); sb = ldc2(x.rS, (64 * c + 48 + t) * 8, W_ST);
      if (__all(sa.x != POISON && sb.x != POISON)) break;
    }
    const float ra = 1.f + rsq(f_(sa.y) + EPS), rb = 1.f + rsq(f_(sb.y) + EPS);
    *lp<float>(M_STM + lane * 4) = f_(sa.x); *lp<float>(M_STA + lane * 4) = ra * rsq(f_(sa.y) * ra * ra + EPS);
    *lp<float>(M_STM + (64 + t) * 4) = f_(sb.x); *lp<float>(M_STA + (64 + t) * 4) = rb * rsq(f_(sb.y) * rb * rb + EPS);
  }
  if (x.ff < 0) top2(x, 0, k1, k2, false); else BAR();
  STAMP(14);
#pragma unroll 1
  for (int sl = 0; sl < 2; ++sl) {
    const int tau = key_tok(sl ? k2 : k1), R = tau - (64 * c - 16);
    if (R < 2 || R >= 80 || (c == 0 && R < 16)) continue;
    const v4u sel = sel_frag(t);
    const unsigned o = (sl * 44 + g) * 1024 + w * 256 + t * 16;
    const v2u xr = *lp<v2u>(XIMG + ((R >> 4) * 16 + 4 * w + (t >> 2)) * 1024 + ((t & 3) * 16 + (R & 15)) * 16 + (g & 1) * 8);
    const v2u st = ldc2(x.rS, tau * 8, W_ST);
    f32x4 d;
    for (int sp = 0; sp < SPIN; ++sp) {
      MEMF();
      v4u y[11];
#pragma unroll
      for (int i = 0; i < 11; ++i) y[i] = ldc4(x.rS, (i == 10 && g == 3) ? OOB : o + i * 4096, W_Y);
      unsigned m = 0;
#pragma unroll
      for (int i = 0; i < 11; ++i) { if (i == 0) mf_vvn<true>(d, sel, y[i]); else mf_vv<false>(d, sel, y[i]); m = max(m, y[i].x); }
      TIE1(7, d);
      if (__all(m != POISON)) break;
    }
    STAMP(15);
    const float m1 = f_(st.x), rs1 = rsq(f_(st.y) + EPS);
    const f32x4 xv = {blo(xr.x), bhi(xr.x), blo(xr.y), bhi(xr.y)};
    const f32x4 x2 = xv * (1.f + rs1) - m1 * rs1 + d;
    const v2u xp = {pk(x2[0], x2[1]), pk(x2[2], x2[3])};
    float s1 = dot2(xp.x, ONE2, dot2(xp.y, ONE2, 0.f)), s2 = dot2(xp.x, xp.x, dot2(xp.y, xp.y, 0.f));
    s1 = x16sum(rowsum(s1)); s2 = x16sum(rowsum(s2));
    if (g < 2) { *lp<v2u>(M_FIX + w * 256 + t * 16 + g * 8) = xp; *lp<f32x2>(M_FST + w * 8) = f32x2{s1, s2}; }
    BAR();
    f32x4 fq, fk, fv;
    {
      v4u xf[4];
      const unsigned fb = M_FIX + g * 16;
#pragma unroll
      for (int s = 0; s < 3; ++s) xf[s] = *lp<v4u>(fb + s * 64);
      sfor<0, 16>([&](auto S) {
        constexpr int s = S.v;
        mf_av<s == 0, AW(s, 0)>(fq, xf[s & 3]);
        if constexpr (s + 3 < 16) xf[(s + 3) & 3] = *lp<v4u>(fb + (s + 3) * 64);
        SB();
        mf_av<s == 0, AW(s, 1)>(fk, xf[s & 3]); mf_va<s == 0, AW(s, 2)>(fv, xf[s & 3]);
        SB();
      });
      TIE3(7, fq, fk, fv);
    }
    const f32x4 sa = *lp<f32x4>(M_FST), sb = *lp<f32x4>(M_FST + 16);
    const float m2 = ((sa[0] + sa[2]) + (sb[0] + sb[2])) * (1.f / 512);
    const float v2 = fmaxf(((sa[1] + sa[3]) + (sb[1] + sb[3])) * (1.f / 512) - m2 * m2, 0.f);
    const bool mt = t == (R & 15);
#pragma unroll
    for (int i = 0; i < 5; ++i)
      if ((R >> 4) == i) {
#pragma unroll
        for (int r = 0; r < 4; ++r) {
          if (i > 0) a.Q[i][r] = mt ? fq[r] : a.Q[i][r];
          a.K[i][r] = mt ? fk[r] : a.K[i][r];
          a.V[i][r] = (4 * g + r == (R & 15)) ? fv[r] : a.V[i][r];
        }
      }
    *lp<float>(M_STM + R * 4) = m2; *lp<float>(M_STA + R * 4) = rsq(v2 + EPS);
    BAR();
  }
  STAMP(16);
  attention(x, a, 1);
  STAMP(17);
#pragma unroll
  for (int T = 0; T < 2; ++T) {
    const f32x2 ps = *lp<f32x2>(M_PS + (16 * T + t) * 8);
    unpack8(xk[T], acc[T][0], acc[T][1]);
    acc[T][0] = acc[T][0] * (1.f + ps[1]) - ps[0] * ps[1]; acc[T][1] = acc[T][1] * (1.f + ps[1]) - ps[0] * ps[1];
  }
#pragma unroll 1
  for (int sl = 0; sl < 2; ++sl) {
    const int R = key_tok(sl ? k2 : k1) - (64 * x.c2 + 32 * x.u);
    if (R < 0 || R >= 32) continue;
    y_slice(x, 0, sl);
    const f32x4 ya = *lp<f32x4>(M_YS + w * 128 + g * 32), yb = *lp<f32x4>(M_YS + w * 128 + g * 32 + 16);
#pragma unroll
    for (int T = 0; T < 2; ++T) { const float f = (16 * T + t == R) ? 1.f : 0.f; acc[T][0] += ya * f; acc[T][1] += yb * f; }
  }
  DRAIN();
  STAMP(18);
  load_o(x, 1, ob);
  STAMP(19);
  BAR();
  outproj(x, WO, ob, acc, wfr, wsum, 1, xk);
  STAMP(21);
  BAR();
  const rsrc_t rOut = mk_rs(out, 2048 * 1024);
  const unsigned oso = (64 * x.c2 + 32 * x.u) * 1024 + n0 * 2;
#pragma unroll
  for (int T = 0; T < 2; ++T) {
    const f32x2 ps = *lp<f32x2>(M_PS + (16 * T + t) * 8);
    unpack8(xk[T], acc[T][0], acc[T][1]);
    acc[T][0] = acc[T][0] * (1.f + ps[1]) - ps[0] * ps[1]; acc[T][1] = acc[T][1] * (1.f + ps[1]) - ps[0] * ps[1];
    __builtin_amdgcn_raw_buffer_store_b128(v4u{pk(acc[T][0][0], acc[T][0][1]), pk(acc[T][0][2], acc[T][0][3]), pk(acc[T][1][0], acc[T][1][1]), pk(acc[T][1][2], acc[T][1][3])},
                                           rOut, (16 * T + t) * 1024 + g * 16, oso, 0);
  }
  STAMP(22);
  if (x.ff >= 0) {
    v4u raw[8]; Ffn F;
    ffn_load_down(x, rD, raw);
    ffn_prep(x, raw, F);
    top2(x, 1, k1, k2, false);
    STAMP(23);
    ffn_run(x, F, 1, k1, k2);
    STAMP(24);
  } else top2(x, 1, k1, k2, true);
  STAMP(25);
#pragma unroll 1
  for (int sl = 0; sl < 2; ++sl) {
    const int R = key_tok(sl ? k2 : k1) - (64 * x.c2 + 32 * x.u);
    if (R < 0 || R >= 32) continue;
    y_slice(x, 1, sl);
    const f32x4 ya = *lp<f32x4>(M_YS + w * 128 + g * 32), yb = *lp<f32x4>(M_YS + w * 128 + g * 32 + 16);
    const f32x4 a0 = (R < 16 ? acc[0][0] : acc[1][0]) + ya, a1 = (R < 16 ? acc[0][1] : acc[1][1]) + yb;
    __builtin_amdgcn_raw_buffer_store_b128(v4u{pk(a0[0], a0[1]), pk(a0[2], a0[3]), pk(a1[0], a1[1]), pk(a1[2], a1[3])},
                                           rOut, t == (R & 15) ? R * 1024 + g * 16 : OOB, oso, 0);
  }
  STAMP(26);
}
}

extern "C" void kernel_launch(void* const* d_in, const int* in_sizes, int n_in, void* d_out, int out_size, void* d_ws, size_t ws_size, hipStream_t stream) {
  fused<<<256, 256, 0, stream>>>((const u16*)d_in[0], (const u16*)d_in[1], (const u16*)d_in[2], (const u16*)d_in[4], (const u16*)d_in[5],
                                 (const u16*)d_in[6], (const u16*)d_in[7], (u16*)d_out, (char*)d_ws);
}
```

```cpp
#include <hip/hip_runtime.h>

namespace {
typedef unsigned short u16;
typedef unsigned long long u64;
typedef unsigned v4u __attribute__((ext_vector_type(4)));
typedef unsigned v2u __attribute__((ext_vector_type(2)));
typedef float f32x4 __attribute__((ext_vector_type(4)));
typedef float f32x2 __attribute__((ext_vector_type(2)));
typedef __bf16 bf16x8 __attribute__((ext_vector_type(8)));
typedef __bf16 bf16x2 __attribute__((ext_vector_type(2)));
typedef short s16x4 __attribute__((ext_vector_type(4)));
typedef __amdgpu_buffer_rsrc_t rsrc_t;
#define LAS __attribute__((address_space(3)))
#define DEV __device__ __forceinline__

#define MAPPING 0

constexpr int W_EPOCH = 0, W_FLAGS = 4096, W_RES = 12288, W_KEY = 16384, W_ST = 32768, W_PST = 65536, W_ST0 = 327680, W_Y = 393216, W_XF = 655360, XF_L = 32768;
constexpr int KEY_L = 8192, ST_L = 16384, PST_L = 131072, Y_L = 98304;
constexpr int W_O = 1 << 20, W_X = 5 << 20, BIG_L = 2 << 20, W_END = 9 << 20;
constexpr int W_DBG = 16 << 20;
constexpr unsigned OOB = 0x7ff00000u;
constexpr unsigned POISON = 0xFFFFFFFFu;
constexpr int NFF = 43;
constexpr int SPIN = 1 << 15;
constexpr float EPS = 1e-5f;
constexpr int XIMG = 8192, RING = 90112, LDS_BYTES = 163840;
constexpr int M_STM = 0, M_STA = 320, M_RS = 640, M_RED = 1024, M_TOP = 3328, M_DUMP = 3392, M_HF = 3584, M_BC = 4096, M_BP = 5120, M_FST = 6144, M_YS = 6400, M_DBG = 6912, M_CS = 7168, M_DBG2 = 7936;
constexpr int WO_X = XIMG + 24576, O_HI = RING + 65536;

__shared__ __attribute__((aligned(16))) char smem[LDS_BYTES];
template <class T> DEV LAS T* lp(unsigned off) { return (LAS T*)((LAS char*)smem + off); }

#define MEMF() asm volatile("" ::: "memory")
#define WAITV(n) asm volatile("s_waitcnt vmcnt(" #n ")" ::: "memory")
#define WAITL() asm volatile("s_waitcnt lgkmcnt(0)" ::: "memory")
#define DRAIN() WAITV(0)
#define BAR() do { WAITL(); __builtin_amdgcn_s_barrier(); MEMF(); } while (0)
#define SB() __builtin_amdgcn_sched_barrier(0)
template <int N> DEV void waitv() { asm volatile("s_waitcnt vmcnt(%0)" ::"n"(N) : "memory"); }

#define STAMP(i)
#define FS(i)

DEV v4u mk_srd(const void* p, unsigned bytes) { const u64 a = (u64)p; return v4u{(unsigned)a, (unsigned)(a >> 32) & 0xffffu, bytes, 0x00020000u}; }
DEV rsrc_t mk_rs(const void* p, unsigned bytes) { return __builtin_amdgcn_make_buffer_rsrc((void*)p, 0, bytes, 0x00020000); }
DEV void set_m0(unsigned v) { asm volatile("s_mov_b32 m0, %0\n\ts_nop 0" ::"s"(v) : "memory"); }
template <int ADD> DEV void set_m0a(unsigned v) { asm volatile("s_add_i32 m0, %0, %1\n\ts_nop 0" ::"s"(v), "n"(ADD) : "memory", "scc"); }
template <int OFF> DEV void dma(v4u srd, unsigned vo, unsigned so) { asm volatile("buffer_load_dwordx4 %0, %1, %2 offen offset:%c3 lds" ::"v"(vo), "s"(srd), "s"(so), "n"(OFF) : "memory"); }
template <int OFF> DEV void dmac(v4u srd, unsigned vo, unsigned so) { asm volatile("buffer_load_dwordx4 %0, %1, %2 offen offset:%c3 sc1 lds" ::"v"(vo), "s"(srd), "s"(so), "n"(OFF) : "memory"); }
template <int LI, int SI> DEV void dma_at(v4u srd, unsigned vo, unsigned lb, unsigned sb) {
  unsigned t;
  asm volatile("s_add_i32 m0, %3, %5\n\ts_add_i32 %0, %4, %6\n\tbuffer_load_dwordx4 %1, %2, %0 offen lds"
               : "=&s"(t) : "v"(vo), "s"(srd), "s"(lb), "s"(sb), "n"(LI), "n"(SI) : "memory", "scc");
}
DEV void dma4c(v4u srd, unsigned vo, unsigned lb, unsigned so) { set_m0(lb); dmac<0>(srd, vo, so); dmac<1024>(srd, vo, so); dmac<2048>(srd, vo, so); dmac<3072>(srd, vo, so); }
DEV v4u ldc4(rsrc_t r, unsigned vo, unsigned so = 0) { return __builtin_amdgcn_raw_buffer_load_b128(r, vo, so, 16); }
DEV v2u ldc2(rsrc_t r, unsigned vo, unsigned so = 0) { return __builtin_amdgcn_raw_buffer_load_b64(r, vo, so, 16); }
DEV unsigned ldc1(rsrc_t r, unsigned vo, unsigned so = 0) { return __builtin_amdgcn_raw_buffer_load_b32(r, vo, so, 16); }
DEV void stc4(rsrc_t r, unsigned vo, v4u v, unsigned so = 0) { __builtin_amdgcn_raw_buffer_store_b128(v, r, vo, so, 16); }
DEV void stc2(rsrc_t r, unsigned vo, v2u v, unsigned so = 0) { __builtin_amdgcn_raw_buffer_store_b64(v, r, vo, so, 16); }
DEV v4u ldp4(rsrc_t r, unsigned vo, unsigned so = 0) { return __builtin_amdgcn_raw_buffer_load_b128(r, vo, so, 0); }

#define MF_(op, pre, d, a, b, ca, cb) asm volatile(pre op " %0, %1, %2, %0" : "+v"(d) : ca(a), cb(b))
#define MFZ_(op, pre, d, a, b, ca, cb) asm volatile(pre op " %0, %1, %2, 0" : "=&v"(d) : ca(a), cb(b))
#define MFC_(op, pre, d, a, b, c) asm volatile(pre op " %0, %1, %2, %3" : "=&v"(d) : "v"(a), "v"(b), "v"(c))
#define M32 "v_mfma_f32_16x16x32_bf16"
#define M16 "v_mfma_f32_16x16x16_bf16"
#define NOP2 "s_nop 1\n\t"
constexpr int AW(int s, int p) { return (s * 3 + p) * 4; }
constexpr int AO(int j, int ct) { return 192 + (j * 2 + ct) * 4; }
template <bool Z, int A> DEV void mf_av(f32x4& d, const v4u& b) {
  if constexpr (Z) asm volatile(M32 " %0, a[%c2:%c3], %1, 0" : "=&v"(d) : "v"(b), "n"(A), "n"(A + 3));
  else asm volatile(M32 " %0, a[%c2:%c3], %1, %0" : "+v"(d) : "v"(b), "n"(A), "n"(A + 3));
}
template <bool Z, int A> DEV void mf_va(f32x4& d, const v4u& b) {
  if constexpr (Z) asm volatile(M32 " %0, %1, a[%c2:%c3], 0" : "=&v"(d) : "v"(b), "n"(A), "n"(A + 3));
  else asm volatile(M32 " %0, %1, a[%c2:%c3], %0" : "+v"(d) : "v"(b), "n"(A), "n"(A + 3));
}
template <int A, int O0, int O1> DEV void trd_a(unsigned base) {
  static_assert(O0 < 65536 && O1 < 65536, "DS offset");
  asm volatile("ds_read_b64_tr_b16 a[%c1:%c2], %0 offset:%c5\n\tds_read_b64_tr_b16 a[%c3:%c4], %0 offset:%c6"
               ::"v"(base), "n"(A), "n"(A + 1), "n"(A + 2), "n"(A + 3), "n"(O0), "n"(O1) : "memory");
}
#define A8(n) "a" #n "0", "a" #n "1", "a" #n "2", "a" #n "3", "a" #n "4", "a" #n "5", "a" #n "6", "a" #n "7", "a" #n "8", "a" #n "9"
#define CLAIM_AGPRS() asm volatile("" ::: A8(), A8(1), A8(2), A8(3), A8(4), A8(5), A8(6), A8(7), A8(8), A8(9), A8(10), A8(11), A8(12), A8(13), A8(14), A8(15), A8(16), \
    A8(17), A8(18), A8(19), A8(20), A8(21), A8(22), A8(23), A8(24), "a250", "a251", "a252", "a253", "a254", "a255")
template <bool Z> DEV void mf_vv(f32x4& d, const v4u& a, const v4u& b) { if constexpr (Z) MFZ_(M32, "", d, a, b, "v", "v"); else MF_(M32, "", d, a, b, "v", "v"); }
template <bool Z> DEV void mf_vvn(f32x4& d, const v4u& a, const v4u& b) { if constexpr (Z) MFZ_(M32, NOP2, d, a, b, "v", "v"); else MF_(M32, NOP2, d, a, b, "v", "v"); }
#define TIE1(n, a) asm volatile("s_nop " #n : "+v"(a))
#define TIE2(n, a, b) asm volatile("s_nop " #n : "+v"(a), "+v"(b))
#define TIE3(n, a, b, c) asm volatile("s_nop " #n : "+v"(a), "+v"(b), "+v"(c))
#define TIE4(n, a, b, c, d) asm volatile("s_nop " #n : "+v"(a), "+v"(b), "+v"(c), "+v"(d))
#define TIE0_2(a, b) asm volatile("" : "+v"(a), "+v"(b))
#define TIE0_4(a, b, c, d) asm volatile("" : "+v"(a), "+v"(b), "+v"(c), "+v"(d))
DEV v2u trd(unsigned off) { return __builtin_bit_cast(v2u, __builtin_amdgcn_ds_read_tr16_b64_v4i16(lp<s16x4>(off))); }
DEV v4u trd2(unsigned o0, unsigned o1) { const v2u a = trd(o0), b = trd(o1); return v4u{a.x, a.y, b.x, b.y}; }
DEV unsigned pk(float a, float b) { return __builtin_bit_cast(unsigned, __builtin_convertvector((f32x2{a, b}), bf16x2)); }
DEV float blo(unsigned v) { return __builtin_bit_cast(float, v << 16); }
DEV float bhi(unsigned v) { return __builtin_bit_cast(float, v & 0xffff0000u); }
DEV float dot2(unsigned a, unsigned b, float c) { return __builtin_amdgcn_fdot2_f32_bf16(__builtin_bit_cast(bf16x2, a), __builtin_bit_cast(bf16x2, b), c, false); }
DEV float f_(unsigned v) { return __builtin_bit_cast(float, v); }
DEV unsigned u_(float v) { return __builtin_bit_cast(unsigned, v); }
template <int C> DEV unsigned dppu(unsigned v) { return (unsigned)__builtin_amdgcn_update_dpp(0, (int)v, C, 0xF, 0xF, true); }
template <int C> DEV float dppf(float v) { return f_(dppu<C>(u_(v))); }
DEV float rowsum(float v) { v += dppf<0x128>(v); v += dppf<0x124>(v); v += dppf<0x122>(v); v += dppf<0x121>(v); return v; }
DEV unsigned rowmaxu(unsigned v) { v = max(v, dppu<0x128>(v)); v = max(v, dppu<0x124>(v)); v = max(v, dppu<0x122>(v)); v = max(v, dppu<0x121>(v)); return v; }
DEV float x16sum(float v) { auto r = __builtin_amdgcn_permlane16_swap(u_(v), u_(v), false, false); return f_(r[0]) + f_(r[1]); }
DEV float x32sum(float v) { auto r = __builtin_amdgcn_permlane32_swap(u_(v), u_(v), false, false); return f_(r[0]) + f_(r[1]); }
DEV float x16max(float v) { auto r = __builtin_amdgcn_permlane16_swap(u_(v), u_(v), false, false); return fmaxf(f_(r[0]), f_(r[1])); }
DEV float x32max(float v) { auto r = __builtin_amdgcn_permlane32_swap(u_(v), u_(v), false, false); return fmaxf(f_(r[0]), f_(r[1])); }
DEV unsigned x16maxu(unsigned v) { auto r = __builtin_amdgcn_permlane16_swap(v, v, false, false); return max((unsigned)r[0], (unsigned)r[1]); }
DEV unsigned x32maxu(unsigned v) { auto r = __builtin_amdgcn_permlane32_swap(v, v, false, false); return max((unsigned)r[0], (unsigned)r[1]); }
DEV bool any(bool c) { return __builtin_amdgcn_ballot_w64(c) != 0; }
#define RARE(c) __builtin_expect(!!(c), 0)
DEV void swap4(f32x4& a, f32x4& b) {
  float a0 = a[0], a1 = a[1], a2 = a[2], a3 = a[3], b0 = b[0], b1 = b[1], b2 = b[2], b3 = b[3];
  asm volatile("v_swap_b32 %0, %4\n\tv_swap_b32 %1, %5\n\tv_swap_b32 %2, %6\n\tv_swap_b32 %3, %7" : "+v"(a0), "+v"(a1), "+v"(a2), "+v"(a3), "+v"(b0), "+v"(b1), "+v"(b2), "+v"(b3));
  a = f32x4{a0, a1, a2, a3}; b = f32x4{b0, b1, b2, b3};
}
template <int C, int RM> DEV unsigned dmax(unsigned v) { return max(v, (unsigned)__builtin_amdgcn_update_dpp(0, (int)v, C, RM, 0xF, false)); }
template <bool HALF> DEV unsigned wave_max(unsigned v) {
  v = dmax<0x111, 0xF>(v); v = dmax<0x112, 0xF>(v); v = dmax<0x114, 0xF>(v); v = dmax<0x118, 0xF>(v); v = dmax<0x142, 0xA>(v);
  if constexpr (HALF) return (unsigned)__builtin_amdgcn_readlane((int)v, 31);
  else return (unsigned)__builtin_amdgcn_readlane((int)dmax<0x143, 0xC>(v), 63);
}
template <int C, int RM> DEV void pair_step(unsigned& hi, unsigned& lo) {
  const unsigned oh = (unsigned)__builtin_amdgcn_update_dpp(0, (int)hi, C, RM, 0xF, false), ol = dmax<C, RM>(lo);
  lo = max(min(hi, oh), ol); hi = max(hi, oh);
}
template <bool HALF> DEV void wave_top2(unsigned hi, unsigned lo, unsigned& k1, unsigned& k2) {
  pair_step<0x111, 0xF>(hi, lo); pair_step<0x112, 0xF>(hi, lo); pair_step<0x114, 0xF>(hi, lo); pair_step<0x118, 0xF>(hi, lo); pair_step<0x142, 0xA>(hi, lo);
  if constexpr (!HALF) pair_step<0x143, 0xC>(hi, lo);
  k1 = (unsigned)__builtin_amdgcn_readlane((int)hi, HALF ? 31 : 63); k2 = (unsigned)__builtin_amdgcn_readlane((int)lo, HALF ? 31 : 63);
}
#define POLL(load, bad) do { _Pragma("unroll 1") for (int sp_ = 0; sp_ < SPIN; ++sp_) { MEMF(); load; if (!any(bad)) break; } } while (0)
DEV unsigned rfl(unsigned v) { return (unsigned)__builtin_amdgcn_readfirstlane((int)v); }
DEV float rsq(float v) { return __builtin_amdgcn_rsqf(v); }
DEV float rcp(float v) { return __builtin_amdgcn_rcpf(v); }
DEV float ex2(float v) { return __builtin_amdgcn_exp2f(v); }
DEV bool isnan_(float v) { return v != v; }
constexpr unsigned ONE2 = 0x3F803F80u;
constexpr float LOG2E = 1.4426950408889634f;
DEV float max2(float a, float b) { return __builtin_elementwise_maximum(a, b); }
DEV float max3(float a, float b, float c) { return max2(max2(a, b), c); }
template <int O> DEV void ldx(v4u& d, unsigned base) { static_assert(O < 65536, "DS offset"); asm volatile("ds_read_b128 %0, %1 offset:%c2" : "=v"(d) : "v"(base), "n"(O) : "memory"); }
template <int N> DEV void waitl() { asm volatile("s_waitcnt lgkmcnt(%0)" ::"n"(N) : "memory"); }
DEV void sw16(float& a, float& b) { auto r = __builtin_amdgcn_permlane16_swap(u_(a), u_(b), false, false); a = f_(r[0]); b = f_(r[1]); }
DEV void sw32(float& a, float& b) { auto r = __builtin_amdgcn_permlane32_swap(u_(a), u_(b), false, false); a = f_(r[0]); b = f_(r[1]); }
DEV v4u sw16p(v2u a, v2u b) { auto r = __builtin_amdgcn_permlane16_swap(a.x, b.x, false, false); auto s = __builtin_amdgcn_permlane16_swap(a.y, b.y, false, false); return v4u{r[0], s[0], r[1], s[1]}; }
DEV void unpack8(v4u v, f32x4& a, f32x4& b) { a = f32x4{blo(v.x), bhi(v.x), blo(v.y), bhi(v.y)}; b = f32x4{blo(v.z), bhi(v.z), blo(v.w), bhi(v.w)}; }
DEV v4u pack8(const f32x4& a, const f32x4& b) { return v4u{pk(a[0], a[1]), pk(a[2], a[3]), pk(b[0], b[1]), pk(b[2], b[3])}; }
template <int S> struct IC { static constexpr int v = S; };
template <int A, int B, class F> DEV void sfor(F&& f) { if constexpr (A < B) { f(IC<A>{}); sfor<A + 1, B>(f); } }

template <int ADD> DEV void m0_add(unsigned v) { asm volatile("s_add_i32 m0, %0, %1" ::"s"(v), "n"(ADD) : "memory", "scc"); }
template <int V> DEV unsigned s_lit() { unsigned r; asm volatile("s_mov_b32 %0, %1" : "=s"(r) : "n"(V)); return r; }
template <int ADD> DEV unsigned s_plus(unsigned v) { unsigned r; asm volatile("s_add_i32 %0, %1, %2" : "=s"(r) : "s"(v), "n"(ADD) : "scc"); return r; }
template <int V, int L> DEV void waitvl() { asm volatile("s_waitcnt vmcnt(%0) lgkmcnt(%1)" ::"n"(V), "n"(L) : "memory"); }
constexpr int p1_extra(int s) { return s == 6 ? 2 : (s == 3 ? 1 : 0); }
constexpr int p1_main(int s) { return s < 12 ? 4 : 3; }
constexpr int p1_wait(int s) {
  int total = 22, pos = 22;
  const int cp[3] = {6, 11, 18};
  for (int k = 0; k < s; ++k) total += p1_main(k) + p1_extra(k);
  if (s < 3) pos = cp[s];
  else {
    for (int k = 0; k < s - 3; ++k) pos += p1_main(k) + p1_extra(k);
    pos += p1_main(s - 3);
  }
  return total - pos;
}
#define DP0 0
#define DP1 6
#define DP2 12
#define XP0 3
#define XP1 9
#define CONC 0
#define CPF 1
#define PFJ 2
#define TLBT 0
#define YS 0
#define PFM 6
#define PEND 1
#define NAP 4
#define NAPO 0
#define PENDO 1
#define DLY0 0
#define DLY1 8
#define RDM 0
#define DXF 8
#define NAPY 0
#define DYF 0
#define NAPF 0
#define DFIN 0
#define NAPL 3
#define PF2 1
#define ABL 0
struct Ctx {
  int lane, w, t, g, c, hg, u, q, ff;
  rsrc_t rS;
  v4u sS;
  DEV void fresh() { asm volatile("" : "+v"(lane)); t = lane & 15; g = lane >> 4; }
};
struct Acc { f32x4 Q[5], K[5], V[5]; };
struct F1 { float v; float pad[3]; };
struct Pre { F1 m[5], s[5], cqr; f32x4 m4[5], s4[5], bp, bc, b0; unsigned slo, shi; };
DEV void pre_masks(const Ctx& x, Pre& p) {
#pragma unroll
  for (int r = 0; r < 4; ++r) {
    const int kk = 4 * x.g + r; const bool par = ((x.t ^ r) & 1) == 0;
    p.bc[r] = (par && kk <= x.t) ? 0.f : -1e30f;
    p.bp[r] = (par && kk >= x.t + 2) ? 0.f : -1e30f;
    p.b0[r] = x.c == 0 ? -1e30f : p.bp[r];
  }
  *lp<f32x4>(M_BC + x.lane * 16) = p.bc; *lp<f32x4>(M_BP + x.lane * 16) = p.bp;
  p.slo = (x.t & 1) ? 0x01000c0cu : 0x0c0c0100u; p.shi = p.slo + ((x.t & 1) ? 0x02020000u : 0x00000202u);
  asm volatile("" : "+v"(p.bc), "+v"(p.bp), "+v"(p.b0), "+v"(p.slo), "+v"(p.shi));
}
template <int N> DEV void pre_stat(const Ctx& x, Pre& p) {
  constexpr int i = N >> 2, k = N & 3;
  if constexpr (N == 20) p.cqr.v = *lp<float>(M_STA + 64 + x.lane * 4);
  else if constexpr (k == 0) p.m[i].v = *lp<float>(M_STM + 64 * i + x.t * 4);
  else if constexpr (k == 1) p.s[i].v = *lp<float>(M_STA + 64 * i + x.t * 4);
  else if constexpr (k == 2) p.m4[i] = *lp<f32x4>(M_STM + 64 * i + x.g * 16);
  else p.s4[i] = *lp<f32x4>(M_STA + 64 * i + x.g * 16);
}

struct Pk { v2u Q[5], K[5], V[5]; };
template <class F> DEV void att_prep(const Acc& a, const Pre& p, const f32x4 csq, const f32x4 csk, const float csv, Pk& k, F&& fill) {
  sfor<0, 5>([&](auto I) {
    constexpr int i = I.v;
    const f32x4 kk = (a.K[i] - p.m[i].v * csk) * p.s[i].v, vv = (a.V[i] - p.m4[i] * csv) * p.s4[i];
    k.K[i] = v2u{pk(kk[0], kk[1]), pk(kk[2], kk[3])}; k.V[i] = v2u{pk(vv[0], vv[1]), pk(vv[2], vv[3])};
    if constexpr (i > 0) { const f32x4 qq = a.Q[i] - p.m[i].v * csq; k.Q[i] = v2u{pk(qq[0], qq[1]), pk(qq[2], qq[3])}; asm volatile("" : "+v"(k.Q[i])); }
    asm volatile("" : "+v"(k.K[i]), "+v"(k.V[i]));
    fill(I);
  });
}
template <int A, int B, class F> DEV void att_finish(const Ctx& x, const Pk& k, const Pre& p, int L, F&& fill) {
  f32x4 sp[5], sc[5], o[5], l[5], e[5];
  sfor<A, B>([&](auto I) {
    constexpr int i = I.v;
    asm volatile(NOP2 M16 " %0, %2, %4, %5\n\t" M16 " %1, %3, %4, %6" : "=&v"(sp[i]), "=&v"(sc[i]) : "v"(k.K[i - 1]), "v"(k.K[i]), "v"(k.Q[i]), "v"(i == 1 ? p.b0 : p.bp), "v"(p.bc));
    fill(IC<i - 1>{});
    SB();
  });
  v4u VV[5];
  float mx[5];
  sfor<A, B>([&](auto I) {
    constexpr int i = I.v;
    VV[i] = v4u{k.V[i - 1].x, k.V[i - 1].y, k.V[i].x, k.V[i].y};
    if constexpr (i == B - 1) asm volatile("s_nop 3" : "+v"(sp[i]), "+v"(sc[i]), "+v"(VV[i]) : "v"(p.b0), "v"(p.bp), "v"(p.bc));
    else if constexpr (i == B - 2 && i == A) asm volatile("s_nop 3" : "+v"(sp[i]), "+v"(sc[i]), "+v"(VV[i]));
    else asm volatile("" : "+v"(sp[i]), "+v"(sc[i]), "+v"(VV[i]));
    e[i] = f32x4{max2(sp[i][0], sp[i][1]), max2(sp[i][2], sp[i][3]), max2(sc[i][0], sc[i][1]), max2(sc[i][2], sc[i][3])};
    mx[i] = max2(max3(e[i][0], e[i][1], e[i][2]), e[i][3]);
  });
  constexpr float C = 0.25f * LOG2E;
  float cm[5];
  sw16(mx[A], mx[A + 1]);
  float ab = max2(mx[A], mx[A + 1]);
  if constexpr (B - A == 4) {
    sw16(mx[3], mx[4]);
    float cd = max2(mx[3], mx[4]);
    sw32(ab, cd);
    cm[1] = (p.cqr.v * -C) * max2(ab, cd); cm[3] = cm[1];
    sw32(cm[1], cm[3]);
    cm[2] = cm[1]; cm[4] = cm[3];
    sw16(cm[1], cm[2]); sw16(cm[3], cm[4]);
  } else {
    float cd = ab;
    sw32(ab, cd);
    cm[A] = (((x.g & 1) ? p.s[A + 1].v : p.s[A].v) * -C) * max2(ab, cd); cm[A + 1] = cm[A];
    sw16(cm[A], cm[A + 1]);
  }
#pragma unroll
  for (int i = A; i < B; ++i) { e[i] = e[i] * (p.s[i].v * C) + cm[i]; asm volatile("" : "+v"(e[i])); }
  SB();
  const v4u ones = {ONE2, ONE2, ONE2, ONE2};
  sfor<A, B>([&](auto I) {
    constexpr int i = I.v;
    const unsigned c0 = pk(ex2(e[i][0]), ex2(e[i][1])), c1 = pk(ex2(e[i][2]), ex2(e[i][3]));
    const v4u P = {__builtin_amdgcn_perm(0u, c0, p.slo), __builtin_amdgcn_perm(0u, c0, p.shi), __builtin_amdgcn_perm(0u, c1, p.slo), __builtin_amdgcn_perm(0u, c1, p.shi)};
    asm volatile(NOP2 M32 " %0, %2, %4, 0\n\t" M32 " %1, %3, %4, 0" : "=&v"(l[i]), "=&v"(o[i]) : "v"(ones), "v"(VV[i]), "v"(P));
    fill(IC<3 + i>{});
    SB();
  });
  const unsigned ob = W_O + L * BIG_L + (x.c * 64 + 4 * x.hg + x.w) * 1024;
  v2u Op[5];
  sfor<A, B>([&](auto I) {
    constexpr int i = I.v;
    if constexpr (i == B - 1 && B - A == 2) TIE2(3, o[i], l[i]); else TIE0_2(o[i], l[i]);
    const f32x4 on = o[i] * rcp(l[i][0]);
    Op[i] = v2u{pk(on[0], on[1]), pk(on[2], on[3])};
    if constexpr (!(i & 1)) stc4(x.rS, x.lane * 16, sw16p(Op[i - 1], Op[i]), ob + (i / 2 - 1) * 32768);
    SB();
  });
}

DEV void ld_stats(const Ctx& x, unsigned so, v2u& s0, v2u& s1) {
  asm volatile("buffer_load_dwordx2 %0, %2, %3, %4 offen sc1\n\tbuffer_load_dwordx2 %1, %2, %3, %4 offen offset:128 sc1" : "=&v"(s0), "=&v"(s1) : "v"(x.t * 8), "s"(x.sS), "s"(so) : "memory");
}
DEV void ld_xf(const Ctx& x, unsigned& v) { asm volatile("buffer_load_dword %0, %1, %2, %3 offen sc1" : "=v"(v) : "v"(x.lane * 128), "s"(x.sS), "s"(W_XF + x.w * 8192) : "memory"); }
DEV void ld_key(const Ctx& x, v2u& ky) { asm volatile("buffer_load_dwordx2 %0, %1, %2, %3 offen sc1" : "=v"(ky) : "v"(x.lane * 128), "s"(x.sS), "s"(W_KEY) : "memory"); }
DEV void ld_stat80(const Ctx& x, unsigned sa_o, unsigned sb_o, v2u& sa, v2u& sb) {
  asm volatile("buffer_load_dwordx2 %0, %2, %4, %5 offen sc1\n\tbuffer_load_dwordx2 %1, %3, %4, %5 offen sc1" : "=&v"(sa), "=&v"(sb) : "v"(sa_o), "v"(sb_o), "s"(x.sS), "s"(W_ST) : "memory");
}
DEV void pin_f(float& a) { asm volatile("" : "+v"(a)); }
DEV void pin_f2(float& a, float& b) { asm volatile("" : "+v"(a)); asm volatile("" : "+v"(b)); }
DEV void tie_nop(f32x4& a) { TIE1(7, a); }
DEV void pin_s(int& v) { asm volatile("" : "+s"(v)); }
DEV void pin_v(unsigned& v) { asm volatile("" : "+v"(v)); }
DEV void ld_flags(const Ctx& x, unsigned o, v2u& f) { asm volatile("buffer_load_dwordx2 %0, %1, %2, %3 offen sc1" : "=v"(f) : "v"(o), "s"(x.sS), "s"(W_FLAGS) : "memory"); }
DEV void tie4(v4u& a, v4u& b, v4u& c, v4u& d) { TIE0_4(a, b, c, d); }
DEV void tie1(v2u& a) { asm volatile("" : "+v"(a)); }
DEV void tie2(v2u& a, v2u& b) { TIE0_2(a, b); }
DEV unsigned make_key(float r, unsigned low) { const unsigned u = u_(r); return ((u ^ ((unsigned)((int)u >> 31) | 0x80000000u)) & ~0x7FFu) | low; }
constexpr int M_LST = M_BC;
struct Late { int lc[4]; };
DEV void top2_late(const Ctx& x, const Late& lt, const float wsum, unsigned& k1, unsigned& k2) {
  const int mine = x.w == 0 ? lt.lc[0] : (x.w == 1 ? lt.lc[1] : (x.w == 2 ? lt.lc[2] : lt.lc[3])), cl = x.lane >> 1;
  unsigned po = mine < 0 ? OOB : mine * 4096 + (x.lane >> 5) * 2048 + (x.lane & 31) * 16, low = 2047 - (mine * 64 + x.lane);
  unsigned ko = (x.w != 0 || cl == lt.lc[0] || cl == lt.lc[1] || cl == lt.lc[2] || cl == lt.lc[3]) ? OOB : x.lane * 128;
  pin_v(po); pin_v(low); pin_v(ko);
  v2u k = {0u, 0u}; v4u a, b, c, e;
  POLL(if (NAPL) __builtin_amdgcn_s_sleep(NAPL); const v2u kn = ldc2(x.rS, ko, W_KEY + KEY_L);
       a = ldc4(x.rS, po, W_PST + PST_L); b = ldc4(x.rS, po, W_PST + PST_L + 512); c = ldc4(x.rS, po, W_PST + PST_L + 1024); e = ldc4(x.rS, po, W_PST + PST_L + 1536);
       const bool kp = kn.x == POISON; k = kp ? k : (k | kn); ko = kp ? ko : OOB,
       max(max(max(a.w, b.w), max(c.w, e.w)), kn.x) == POISON);
  const f32x4 z = (__builtin_bit_cast(f32x4, a) + __builtin_bit_cast(f32x4, b)) + (__builtin_bit_cast(f32x4, c) + __builtin_bit_cast(f32x4, e));
  const float m = z[0] * (1.f / 512), var = fmaxf(z[2] * (1.f / 512) - m * m, 0.f);
  const unsigned kk = mine < 0 ? 0u : make_key(rsq(var + EPS) * (z[1] - m * wsum), low);
  *lp<f32x2>(M_LST + x.w * 512 + x.lane * 8) = f32x2{m, var};
  wave_top2<false>(max(kk, k.x), max(min(kk, k.x), k.y), k1, k2);
  *lp<v2u>(M_TOP + x.w * 8) = v2u{k1, k2};
  BAR();
  const v4u p = *lp<v4u>(M_TOP), q = *lp<v4u>(M_TOP + 16);
  const unsigned h0 = max(p.x, p.z), h1 = max(q.x, q.z), l0 = max(min(p.x, p.z), max(p.y, p.w)), l1 = max(min(q.x, q.z), max(q.y, q.w));
  k1 = rfl(max(h0, h1)); k2 = rfl(max(min(h0, h1), max(l0, l1)));
}
DEV int key_tok(unsigned k) { return 2047 - (int)(k & 0x7FFu); }
DEV float key_sig(unsigned k) {
  unsigned u = (k & ~0x7FFu) | 0x400u;
  u = (u & 0x80000000u) ? (u ^ 0x80000000u) : ~u;
  return rcp(1.f + ex2(-LOG2E * f_(u)));
}

struct Ffn { v4u WD[8], G[4][2], xf[4]; v2u st; float sg; unsigned so, ro, yo, hw, hr, xo, b0; };
template <int H> DEV void ffn_load_down_half(const Ctx& x, const v4u sD, v4u (&raw)[8]) {
  asm volatile("buffer_load_dwordx4 %0, %4, %5, %6 offen\n\tbuffer_load_dwordx4 %1, %4, %5, %6 offen offset:1024\n\t"
               "buffer_load_dwordx4 %2, %4, %5, %6 offen offset:2048\n\tbuffer_load_dwordx4 %3, %4, %5, %6 offen offset:3072"
               : "=&v"(raw[4 * H]), "=&v"(raw[4 * H + 1]), "=&v"(raw[4 * H + 2]), "=&v"(raw[4 * H + 3]) : "v"(x.g * 8192 + x.t * 16 + H * 4096), "s"(sD), "s"(x.ff * 32768 + x.w * 256) : "memory");
}
DEV void ffn_load_down(const Ctx& x, const v4u sD, v4u (&raw)[8]) { ffn_load_down_half<0>(x, sD, raw); ffn_load_down_half<1>(x, sD, raw); }
DEV void ffn_down_landed(v4u (&raw)[8]) { TIE0_4(raw[0], raw[1], raw[2], raw[3]); TIE0_4(raw[4], raw[5], raw[6], raw[7]); }
template <int C> DEV void ffn_perm(const v4u (&raw)[8], Ffn& F) {
#pragma unroll
  for (int d = 0; d < 4; ++d) F.WD[C][d] = __builtin_amdgcn_perm(raw[2 * d + 1][C >> 1], raw[2 * d][C >> 1], (C & 1) ? 0x07060302u : 0x05040100u);
  asm volatile("" : "+v"(F.WD[C]));
}
template <int N> DEV void ffn_const(const Ctx& x, Ffn& F) {
  if constexpr (N == 0) { F.ro = M_RED + ((x.g & 1) * 32 + 16 * (x.g >> 1) + x.t) * 16 + 4 * x.w; pin_v(F.ro); }
  if constexpr (N == 1) { F.yo = x.g >= 2 ? OOB : (x.g * 44 + x.ff) * 1024 + x.w * 256 + x.t * 16; pin_v(F.yo); }
  if constexpr (N == 2) { F.hw = M_HF + x.w * 128 + x.lane * 2; pin_v(F.hw); }
  if constexpr (N == 3) { F.hr = M_HF + x.w * 128 + ((x.t >> 2) & 1) * 64 + x.g * 16; pin_v(F.hr); }
  if constexpr (N == 4) { F.xo = x.w * 4096 + x.g * 256; pin_v(F.xo); }
  if constexpr (N == 5) { F.b0 = RING + (128 * x.w + 8 * x.g + (x.t >> 2)) * 64 + (x.g & 1) * 32 + 8 * (x.lane & 3); pin_v(F.b0); }
  if constexpr (N >= 6) { F.xf[N - 6] = v4u{ONE2, ONE2, ONE2, ONE2}; asm volatile("" : "+v"(F.xf[N - 6])); }
}
DEV void ffn_row_addr(const Ctx& x, Ffn& F, int ta, int tb) { F.xo += (x.t & 4) ? (tb >> 4) * 16384 + (tb & 15) * 16 : (ta >> 4) * 16384 + (ta & 15) * 16; pin_v(F.xo); }
template <int S> DEV void ffn_row(const Ctx& x, Ffn& F, int L) {
  asm volatile("s_mov_b64 exec, %4\n\tbuffer_load_dwordx4 %0, %1, %2, %3 offen offset:%c5 sc1\n\ts_mov_b64 exec, -1"
               : "+v"(F.xf[S]) : "v"(F.xo), "s"(x.sS), "s"(W_X + L * BIG_L), "s"(0x1111111111111111ull), "n"(S * 1024) : "memory");
}
DEV void ffn_row_stats(const Ctx& x, Ffn& F, int L, int ta, int tb) {
  F.so = ((x.g & 1) ? tb : ta) * 8;
  asm volatile("buffer_load_dwordx2 %0, %1, %2, %3 offen sc1" : "=v"(F.st) : "v"(F.so), "s"(x.sS), "s"(W_ST + L * ST_L) : "memory");
}
DEV void ffn_gate_in(Ffn& F) {
#pragma unroll
  for (int s = 0; s < 4; ++s)
#pragma unroll
    for (int m = 0; m < 2; ++m) F.G[s][m] = trd2((F.b0 ^ (32 * m)) + s * 2048, (F.b0 ^ (32 * m)) + s * 2048 + 256);
}
template <class G> DEV void ffn_core(const Ctx& x, Ffn& F, int L, G&& fill) {
#define ROW_BAD (max(max(max(F.xf[0].x, F.xf[1].x), max(F.xf[2].x, F.xf[3].x)), F.st.x) == POISON)
  if (RARE(any(ROW_BAD)))
    POLL(ffn_row<0>(x, F, L); ffn_row<1>(x, F, L); ffn_row<2>(x, F, L); ffn_row<3>(x, F, L); const v2u sn = ldc2(x.rS, F.so, W_ST + L * ST_L); DRAIN(); tie4(F.xf[0], F.xf[1], F.xf[2], F.xf[3]);
         F.st = F.st.x == POISON ? sn : F.st, ROW_BAD);
  const bool hi = x.g >= 2;
  f32x4 d[4]; v4u U[4][2];
  sfor<0, 4>([&](auto S) {
    constexpr int s = S.v;
    mf_vv<s == 0>(d[0], F.xf[s], F.G[s][0]);
    U[s][0] = trd2(F.b0 + 32768 + s * 2048, F.b0 + 32768 + s * 2048 + 256);
    SB();
    mf_vv<s == 0>(d[1], F.xf[s], F.G[s][1]);
    U[s][1] = trd2((F.b0 ^ 32) + 32768 + s * 2048, (F.b0 ^ 32) + 32768 + s * 2048 + 256);
    SB();
  });
  float rs, ru;
  sfor<0, 4>([&](auto S) {
    constexpr int s = S.v;
    mf_vv<s == 0>(d[2], F.xf[s], U[s][0]);
    if constexpr (s == 0) { rs = f_(F.st.y) + EPS; pin_f(rs); }
    if constexpr (s == 1) { rs = rsq(rs); pin_f(rs); }
    if constexpr (s == 2) { ru = rs * F.sg; pin_f(ru); }
    SB();
    mf_vv<s == 0>(d[3], F.xf[s], U[s][1]);
    fill(S);
    SB();
  });
  TIE0_2(d[0], d[1]);
  *lp<float>(F.ro) = ((hi ? d[1][0] : d[0][0]) - f_(F.st.x) * (hi ? d[1][1] : d[0][1])) * rs;
  TIE2(3, d[2], d[3]);
  *lp<float>(F.ro + 1024) = ((hi ? d[3][0] : d[2][0]) - f_(F.st.x) * (hi ? d[3][1] : d[2][1])) * ru;
  fill(IC<4>{});
  BAR();
  const f32x4 g4 = *lp<f32x4>(M_RED + x.lane * 16), u4 = *lp<f32x4>(M_RED + 1024 + x.lane * 16);
  fill(IC<9>{});
  const float gv = (g4[0] + g4[1]) + (g4[2] + g4[3]), uv = (u4[0] + u4[1]) + (u4[2] + u4[3]);
  const float h = gv * rcp(1.f + ex2(-LOG2E * gv)) * uv;
  *lp<u16>(F.hw) = (u16)pk(h, h);
  const v4u hf = *lp<v4u>(F.hr);
  f32x4 y4[8];
  sfor<0, 8>([&](auto C) { mf_vv<true>(y4[C.v], hf, F.WD[C.v]); if constexpr (C.v & 1) fill(IC<5 + C.v / 2>{}); SB(); });
  TIE4(3, y4[0], y4[1], y4[2], y4[3]); TIE4(3, y4[4], y4[5], y4[6], y4[7]);
  stc4(x.rS, F.yo, v4u{pk(y4[0][0], y4[1][0]), pk(y4[2][0], y4[3][0]), pk(y4[4][0], y4[5][0]), pk(y4[6][0], y4[7][0])}, W_Y + L * Y_L);
}
DEV v4u sel_frag(int t) {
  const unsigned v = (t & 1) ? 0x3F800000u : 0x00003F80u;
  return v4u{(t >> 1) == 0 ? v : 0u, (t >> 1) == 1 ? v : 0u, (t >> 1) == 2 ? v : 0u, (t >> 1) == 3 ? v : 0u};
}
DEV void y_slice(const Ctx& x, int L, int slot) {
  const v4u sel = sel_frag(x.t);
  const int p0 = 4 * (x.t >> 2) + x.g;
  const unsigned o = (slot * 44 + p0) * 1024 + (128 * x.q + 32 * x.w) * 2 + (x.t & 3) * 16;
  const unsigned o2 = p0 < NFF - 32 ? o + 32768 : OOB;
  v4u a, b, c;
  if (DYF && x.ff < 0) __builtin_amdgcn_s_sleep(DYF);
  POLL(if (NAPF) __builtin_amdgcn_s_sleep(NAPF); a = ldc4(x.rS, o, W_Y + L * Y_L); b = ldc4(x.rS, o, W_Y + L * Y_L + 16384); c = ldc4(x.rS, o2, W_Y + L * Y_L), max(max(a.x, b.x), c.x) == POISON);
  f32x4 d;
  mf_vvn<true>(d, sel, a); mf_vv<false>(d, sel, b); mf_vv<false>(d, sel, c);
  TIE1(7, d);
#pragma unroll
  for (int r = 0; r < 4; ++r) { d[r] += dppf<0x124>(d[r]); d[r] += dppf<0x128>(d[r]); }
  if (x.g < 2) *lp<f32x4>(M_YS + x.w * 128 + ((x.t & 3) * 8 + 4 * x.g) * 4) = d;
}

struct OReq { unsigned so, lb, sv, smp; };
DEV void o_dma(const Ctx& x, const OReq& r) { dma4c(x.sS, x.lane * 16, r.lb, r.so); dma4c(x.sS, x.lane * 16, r.lb + 4096, r.so + 4096); }
DEV OReq o_plan(const Ctx& x, int L) {
  OReq r;
  r.so = W_O + L * BIG_L + (x.c * 2 + x.u) * 32768 + x.w * 8192; r.lb = x.w < 3 ? XIMG + x.w * 8192 : O_HI;
  r.sv = (x.lane & 7) * 1024 + 1020;
  asm volatile("" : "+s"(r.so), "+s"(r.lb));
  asm volatile("" : "+v"(r.sv));
  return r;
}
DEV void o_issue(const Ctx& x, OReq& r) {
  asm volatile("buffer_load_dword %0, %1, %2, %3 offen sc1" : "=v"(r.smp) : "v"(r.sv), "s"(x.sS), "s"(r.so) : "memory");
  o_dma(x, r);
}
DEV unsigned touch(u64 a, u64 lanes) { unsigned r; asm volatile("s_mov_b64 exec, %2\n\tglobal_load_dword %0, %1, off sc1\n\ts_mov_b64 exec, -1" : "=v"(r) : "v"(a), "s"(lanes) : "memory"); return r; }
constexpr u64 PF_EARLY = PFM == 3 ? 0xffffffull : PFM == 4 ? 0xffffffffffffull : PFM >= 5 ? ~0xffffffffffffull : ~0ull;
template <int N, bool PF> DEV void o_wait(const Ctx& x, OReq& r, u64 pfa, unsigned& pf) {
  waitv<N + 8>();
  asm volatile("" : "+v"(r.smp));
  if (any(r.smp == POISON)) {
    unsigned sv = r.sv;
    if (RDM) {
#pragma unroll 1
      for (int sp = 0; sp < SPIN; ++sp) {
        const unsigned late = (unsigned)__builtin_amdgcn_ballot_w64(r.smp == POISON) & 0xFFu;
        if (late == 0) break;
        if (NAPO) __builtin_amdgcn_s_sleep(NAPO);
        sv = r.smp == POISON ? sv : OOB;
        asm volatile("buffer_load_dword %0, %1, %2, %3 offen sc1" : "=v"(r.smp) : "v"(sv), "s"(x.sS), "s"(r.so) : "memory");
#pragma unroll 1
        for (unsigned m = late; m; m &= m - 1) { const unsigned i = __builtin_ctz(m) * 1024; set_m0(r.lb + i); dmac<0>(x.sS, x.lane * 16, r.so + i); }
        DRAIN();
        pin_v(r.smp);
      }
    } else {
      POLL(__builtin_amdgcn_s_sleep(NAPO); if (PENDO) sv = r.smp == POISON ? sv : OOB; r.smp = ldc1(x.rS, sv, r.so), r.smp == POISON);
      o_dma(x, r);
      if constexpr (N > 0) DRAIN();
    }
  }
  if constexpr (PF) pf = touch(pfa, PF_EARLY);
  waitv<N + (PF ? 1 : 0)>();
}
DEV v4u halves(const Ctx& x, v4u v) { return (x.g & 1) ? v4u{v.z, v.w, v.x, v.y} : v; }
template <int L, int NB, class F>
DEV void outproj(Ctx& x, OReq& rq, const v4u (&WO)[8][2], const f32x4 (&res)[2][2], const v4u wfa, const v4u wfb, const float wsum, v4u (&xk)[2], const u64 pfa, F&& settle) {
  unsigned pf;
  x.fresh();
  const int w = x.w;
  f32x4 acc[2][2];
  const unsigned o0 = XIMG + (x.g >> 1) * 1024 + ((x.g & 1) * 32 + x.t) * 16, o1 = o0 + (O_HI - XIMG);
  if (!L) FS(37);
  o_wait<NB, L == 0 && PFM != 0 && PFM != 2>(x, rq, pfa, pf);
#pragma unroll 1
  for (int tries = 0; tries < 4096; ++tries) {
    if (!L) FS(38);
    BAR();
    STAMP(7 + 20 * L);
    v4u of[3][2];
#pragma unroll
    for (int j = 0; j < 2; ++j) { of[j][0] = *lp<v4u>(o0 + j * 2048); of[j][1] = *lp<v4u>(o0 + j * 2048 + 256); }
    acc[0][0] = res[0][0]; acc[0][1] = res[0][1]; acc[1][0] = res[1][0]; acc[1][1] = res[1][1];
    TIE4(1, acc[0][0], acc[0][1], acc[1][0], acc[1][1]);
    sfor<0, 32>([&](auto J) {
      constexpr int j = J.v >> 1, T = J.v & 1, n = j + 2;
      if constexpr (j < 8) mf_av<false, AO(j, 0)>(acc[T][0], of[j % 3][T]); else mf_vv<false>(acc[T][0], WO[j - 8][0], of[j % 3][T]);
      if constexpr (n < 16) of[n % 3][T] = *lp<v4u>((n < 12 ? o0 + n * 2048 : o1 + (n - 12) * 2048) + T * 256);
      SB();
      if constexpr (j < 8) mf_av<false, AO(j, 1)>(acc[T][1], of[j % 3][T]); else mf_vv<false>(acc[T][1], WO[j - 8][1], of[j % 3][T]);
      SB();
    });
    TIE4(7, acc[0][0], acc[0][1], acc[1][0], acc[1][1]);
    if (!RARE(any(isnan_(acc[0][0][0] + acc[1][0][0])))) break;
    BAR();
    o_dma(x, rq);
    DRAIN();
  }
  STAMP(8 + 12 * L);
  if (!L) FS(39);
  xk[0] = halves(x, pack8(acc[0][0], acc[0][1])); xk[1] = halves(x, pack8(acc[1][0], acc[1][1]));
  f32x4 d;
  asm volatile(NOP2 M32 " %0, %3, %1, 0\n\t" M32 " %0, %4, %2, %0" : "=&v"(d), "+v"(xk[0]), "+v"(xk[1]) : "v"(wfa), "v"(wfb));
  float sa = dot2(xk[0].x, xk[0].x, dot2(xk[0].y, xk[0].y, 0.f)) + dot2(xk[0].z, xk[0].z, dot2(xk[0].w, xk[0].w, 0.f));
  float sb = dot2(xk[1].x, xk[1].x, dot2(xk[1].y, xk[1].y, 0.f)) + dot2(xk[1].z, xk[1].z, dot2(xk[1].w, xk[1].w, 0.f));
  sw16(sa, sb);
  float s2 = x32sum(sa + sb);
  TIE0_2(d, s2);
  *lp<f32x4>(x.g < 2 ? M_RED + (w * 32 + x.lane) * 16 : M_DUMP) = f32x4{d[0], d[1], s2, 0.f};
  const unsigned xo = W_X + L * BIG_L + ((4 * x.c + 2 * x.u) * 16 + 4 * x.q + w) * 1024;
  BAR();
  if (!L) FS(40);
  const int tk = x.lane & 31;
  unsigned pb = W_PST + L * PST_L + (x.c * 2 + x.u) * 2048, fin = w | x.q;
  asm volatile("" : "+s"(pb), "+s"(fin));
  if (w == 0) {
    const f32x4 s = (*lp<f32x4>(M_RED + tk * 16) + *lp<f32x4>(M_RED + 512 + tk * 16)) + (*lp<f32x4>(M_RED + 1024 + tk * 16) + *lp<f32x4>(M_RED + 1536 + tk * 16));
    stc4(x.rS, x.lane < 32 ? x.q * 512 + tk * 16 : OOB, __builtin_bit_cast(v4u, s), pb);
    STAMP(57 + 3 * L);
  }
  if (L == 0 || fin != 0) { stc4(x.rS, x.lane * 16, xk[0], xo); stc4(x.rS, x.lane * 16, xk[1], xo + 16384); }
  if constexpr (L == 0) {
    FS(41);
    settle();
    if constexpr (PFM != 0 && PFM != 2) asm volatile("" : "+v"(pf));
    __builtin_amdgcn_s_barrier();
    MEMF();
    if (w == 1) stc2(x.rS, x.lane == 0 ? ((x.c * 2 + x.u) * 4 + x.q) * 128 : OOB, v2u{0u, 0u}, W_XF);
  }
  if (fin == 0) {
    v4u a, b, c, e;
    if constexpr (L == 1) DRAIN();
    STAMP(58 + 3 * L);
    if (DFIN) __builtin_amdgcn_s_sleep(DFIN);
    asm volatile("buffer_load_dwordx4 %0, %4, %5, %6 offen sc1\n\tbuffer_load_dwordx4 %1, %4, %5, %6 offen offset:512 sc1\n\t"
                 "buffer_load_dwordx4 %2, %4, %5, %6 offen offset:1024 sc1\n\tbuffer_load_dwordx4 %3, %4, %5, %6 offen offset:1536 sc1"
                 : "=&v"(a), "=&v"(b), "=&v"(c), "=&v"(e) : "v"(tk * 16), "s"(x.sS), "s"(pb) : "memory");
    if constexpr (L == 1) { stc4(x.rS, x.lane * 16, xk[0], xo); stc4(x.rS, x.lane * 16, xk[1], xo + 16384); }
    const int tok = x.c * 64 + x.u * 32 + tk;
    unsigned ko = x.lane == 0 ? (x.c * 2 + x.u) * 128 : OOB, so = x.lane < 32 ? tok * 8 : OOB, ti = 2047 - tok;
    asm volatile("" : "+v"(ko), "+v"(so), "+v"(ti));
    waitv<2 * L>();
    TIE0_4(a, b, c, e);
    if (any(max(max(a.w, b.w), max(c.w, e.w)) == POISON))
      POLL(a = ldc4(x.rS, tk * 16, pb); b = ldc4(x.rS, tk * 16 + 512, pb); c = ldc4(x.rS, tk * 16 + 1024, pb); e = ldc4(x.rS, tk * 16 + 1536, pb),
           max(max(a.w, b.w), max(c.w, e.w)) == POISON);
    STAMP(59 + 3 * L);
    const f32x4 z = (__builtin_bit_cast(f32x4, a) + __builtin_bit_cast(f32x4, b)) + (__builtin_bit_cast(f32x4, c) + __builtin_bit_cast(f32x4, e));
    const float m = z[0] * (1.f / 512), var = fmaxf(z[2] * (1.f / 512) - m * m, 0.f);
    const unsigned k = make_key(rsq(var + EPS) * (z[1] - m * wsum), ti);
    unsigned m1, m2;
    wave_top2<true>(k, 0u, m1, m2);
    stc2(x.rS, ko, v2u{m1, m2}, W_KEY + L * KEY_L);
    stc2(x.rS, so, v2u{u_(m), u_(var)}, W_ST + L * ST_L);
    STAMP(5 + L);
  }
}

DEV unsigned small_words(const Ctx& x, int L, unsigned o) { return o + (x.w == 0 ? W_XF : x.w == 1 ? W_PST + L * PST_L : (x.w == 3 ? W_RES : (x.lane < 16 ? W_ST + L * ST_L : W_KEY + L * KEY_L))); }

__global__ void __launch_bounds__(256, 1)
fused(const u16* __restrict__ x0, const u16* __restrict__ wqkv, const u16* __restrict__ wo, const u16* __restrict__ wr, const u16* __restrict__ wg,
      const u16* __restrict__ wu, const u16* __restrict__ wd, u16* __restrict__ out, char* __restrict__ ws) {
  Ctx x;
  const int b = blockIdx.x, tid = threadIdx.x, w = __builtin_amdgcn_readfirstlane(tid >> 6);
  const int xc = b & 7, jb = b >> 3;
  x.lane = tid & 63; x.w = w; x.t = x.lane & 15; x.g = x.lane >> 4;
  x.c = xc * 4 + (jb >> 3); x.hg = jb & 7; x.u = x.hg >> 2; x.q = x.hg & 3;
  { const int f = 6 * xc + jb / 6; x.ff = (jb % 6 == 1 && f < NFF) ? f : -1; }
  asm volatile("" : "+s"(x.ff));
  const int c = x.c, hg = x.hg, gi = 2 * x.c + x.u;
  x.rS = mk_rs(ws, W_END); x.sS = mk_srd(ws, W_END);
  const v4u sX = mk_srd(x0, 2048 * 1024), sW = mk_srd(wqkv, 512 * 3072), sO = mk_srd(wo, 512 * 1024);
  CLAIM_AGPRS();
  STAMP(0);

  Acc a; Pre p;
  v4u xk[2];
  unsigned e0, e1, pso;
  v4u wfa, wfb; float wsum;
  const int n0 = 128 * x.q + 32 * w;
  const v4u pz = {POISON, POISON, POISON, POISON};
  const unsigned ovo = x.g * 1024 + x.q * 256 + (x.t ^ (4 * x.g) ^ (w >> 1)) * 16;
  const unsigned w1 = w * 1024, w3 = w * 3072, w4 = w * 4096;
  auto wo_addr = [&](auto K) {
    m0_add<(K.v < 18 ? RING + K.v * 4096 : WO_X + (K.v - 18) * 4096)>(w1);
    return s_plus<K.v * 16384>(w4);
  };
  auto issue_wo = [&](auto K) { dma<0>(sO, ovo, wo_addr(K)); };
  {
    const int lane = x.lane, t = x.t, g = x.g;
    v2u ep; v4u r0, r1, wr8, wrall; unsigned pf;
    {
      const unsigned ro = (lane >> 5) * 1024 + (lane & 31) * 16, so = (64 * c + 8 * hg + 2 * w) * 1024;
      asm volatile("buffer_load_dwordx4 %1, %4, %5, %6 offen\n\tbuffer_load_dwordx4 %2, %4, %5, %6 offen offset:512\n\tbuffer_load_dwordx2 %0, off, %3, 0 sc1"
                   : "=&v"(ep), "=&v"(r0), "=&v"(r1) : "s"(x.sS), "v"(ro), "s"(sX), "s"(so) : "memory");
      asm volatile("buffer_load_dwordx4 %0, %2, %4, %5 offen\n\tbuffer_load_dwordx4 %1, %3, %4, 0 offen"
                   : "=&v"(wr8), "=&v"(wrall) : "v"(g * 16), "v"(lane * 16), "s"(mk_srd(wr, 1024)), "s"(n0 * 2) : "memory");
    }
    const unsigned wvo = (8 * w + (lane >> 3)) * 3072 + hg * 128 + (((((lane & 7) >> 1) ^ (((lane >> 4) & 1) + 2 * (w & 1))) * 2) + (lane & 1)) * 16;
    auto issue_w = [&](auto S) { set_m0a<RING + 24576 + S.v * 12288>(w3); dma<0>(sW, wvo, S.v * 98304); dma<1024>(sW, wvo, S.v * 98304); dma<2048>(sW, wvo, S.v * 98304); };
    const unsigned xvo = (lane >> 3) * 1024 + ((lane & 7) ^ (4 * (w & 1) + (lane >> 4))) * 16;
    const unsigned xs0 = (64 * c - 16 + 8 * w + ((c == 0 && w < 2) ? 16 : 0)) * 1024, xs1 = (64 * c + 16 + 8 * w) * 1024, xs2 = (64 * c + 48 + 8 * (w & 1)) * 1024;
    const unsigned xl2 = (w & 1) * 1024 + (w >> 1) * 10240, xs3 = xs2 + (w >> 1) * 128;
    auto x_m0 = [&](auto P) {
      constexpr int k = (P.v / 5) * 2 + (P.v % 5 > 2 ? 1 : 0), n = P.v % 5;
      m0_add<XIMG + (n == 2 ? 8192 : (n == 1 || n == 4) ? 4096 : 0) + k * 10240 - k * 128>(n == 2 ? xl2 : w1);
    };
    auto x_dma = [&](auto P) {
      constexpr int k = (P.v / 5) * 2 + (P.v % 5 > 2 ? 1 : 0), n = P.v % 5;
      dma<((ABL & 32) ? k % 3 : k) * 128>(sX, xvo, n == 2 ? xs3 : (n == 1 || n == 4) ? xs1 : xs0);
    };
    auto issue_x = [&](auto P) { x_m0(P); asm volatile("s_nop 0"); x_dma(P); };
    const v4u ones = {ONE2, ONE2, ONE2, ONE2};
    const unsigned onea = XIMG + (lane & 7) * 16;
    sfor<0, 3>(issue_x); issue_w(IC<0>{}); sfor<3, 5>(issue_x); issue_w(IC<1>{});
    pso = OOB;
    if (w == 1) pso = lane < 32 ? gi * 2048 + x.q * 512 + lane * 16 : OOB;
    if (w == 2 && x.q == 0) pso = lane < 16 ? gi * 256 + t * 16 : (lane == 16 ? gi * 128 : OOB);
    if (w == 0) pso = lane == 0 ? (gi * 4 + x.q) * 128 : OOB;
    if (w == 3 && x.ff >= 0 && x.ff < 8) pso = lane == 0 ? x.ff * 128 : OOB;
    pre_masks(x, p);
    u64 ad;
    {
      u64 pc; unsigned len;
      asm volatile("s_getpc_b64 %0\n.Lfused_begin:\n\ts_mov_b32 %1, .Lfused_end-.Lfused_begin" : "=s"(pc), "=s"(len));
      const u64 pa = (u64)wqkv + (b * 48 + w * 12) * 128, pb = (u64)wo + (b * 16 + w * 4 - 12) * 128;
      ad = lane < 16 ? (t < 12 ? pa : pb) + t * 128 : (pc & ~3ull) + min((unsigned)((jb * 4 + w) * 4 + (lane & 3)) * 128, len);
      asm volatile("" : "+v"(ad));
    }
    SB();
    waitv<11>();
    asm volatile("" : "+v"(ep), "+v"(r0), "+v"(r1), "+v"(wr8), "+v"(wrall));
    STAMP(1);
    e0 = rfl(ep.x) + 1u; e1 = rfl(ep.y);
    float s1 = 0.f, s2 = 0.f, s3 = 0.f, s4 = 0.f;
#pragma unroll
    for (int k = 0; k < 4; ++k) { s1 = dot2(r0[k], ONE2, s1); s2 = dot2(r0[k], r0[k], s2); s3 = dot2(r1[k], ONE2, s3); s4 = dot2(r1[k], r1[k], s4); }
    {
      float m = x16sum(rowsum(s1 + s3)) * (1.f / 512), var = fmaxf(x16sum(rowsum(s2 + s4)) * (1.f / 512) - m * m, 0.f);
      stc4(x.rS, (lane & 31) == 0 ? (lane >> 5) * 16 : OOB, v4u{u_(m), u_(rsq(var + EPS)), e0, e1}, W_ST0 + (64 * c + 8 * hg + 2 * w) * 16);
    }
    sfor<5, 8>(issue_x); issue_w(IC<2>{});
    asm volatile("s_mov_b64 exec, %2\n\tglobal_load_dword %0, %1, off sc1\n\ts_mov_b64 exec, -1" : "=v"(pf) : "v"(ad), "n"(0xfffff) : "memory");
    stc4(x.rS, lane * 16, pz, W_O + (c * 64 + 4 * hg + w) * 1024); stc4(x.rS, lane * 16, pz, W_O + (c * 64 + 32 + 4 * hg + w) * 1024);
    stc4(x.rS, small_words(x, 0, pso), pz);
    {
      const v4u zz = {0u, 0u, 0u, 0u};
      wfa = t == 0 ? ones : (t == 1 ? wr8 : zz); wfb = t == 4 ? ones : (t == 5 ? wr8 : zz);
      wsum = x32sum(x16sum(rowsum(dot2(wrall.x, ONE2, dot2(wrall.y, ONE2, 0.f)) + dot2(wrall.z, ONE2, dot2(wrall.w, ONE2, 0.f)))));
      asm volatile("" : "+v"(wfa), "+v"(wfb), "+v"(wsum));
    }
    unsigned xb[2][2];
#pragma unroll
    for (int k = 0; k < 2; ++k) { xb[k][0] = XIMG + t * 128 + ((4 * k + g) ^ (t >> 1)) * 16; xb[k][1] = xb[k][0] + 40960; }
    const unsigned sa_o = max(64 * c - 16 + lane, 0) * 16, sb_o = (64 * c + 48 + t) * 16;
    v4u sa, sb;
    unsigned bad, so = 0, flo = lane == 0 ? W_FLAGS + (b * 4 + w) * 8 : OOB;
    asm volatile("" : "+v"(flo));
    const int qq = t >> 2;
    const unsigned wb = RING + 24576 + g * 3072 + qq * 128 + (w ^ ((qq >> 1) + 2 * (g & 1))) * 32 + 8 * (lane & 3);
    const unsigned ro = XIMG + (2 * x.q + (w >> 1)) * 10240 + (16 + 32 * x.u + t) * 128 + ((4 * (w & 1) + g) ^ (t >> 1)) * 16;
    v4u xf[6];
    sfor<0, 17>([&](auto S) {
      constexpr int s = S.v;
      if constexpr (s < 16) {
        if constexpr (s == 0) { waitv<p1_wait(0)>(); if (w == 0) *lp<v4u>(onea) = ones; WAITL(); so = s_lit<3 * 98304>(); } else waitvl<(ABL & 8) ? 63 : p1_wait(s), 3>();
        if constexpr (!(ABL & 16) || s == 0) __builtin_amdgcn_s_barrier();
        MEMF();
        if constexpr (s == 1) STAMP(32);
      }
      sfor<0, 15>([&](auto N) {
        constexpr int n = N.v, i = n / 3, pp = n % 3, fi = i < 4 ? i : 4 + ((s - 1) & 1);
        if constexpr (s > 0) {
          if constexpr (pp == 0) mf_av<s == 1, AW(s - 1, 1)>(a.K[i], xf[fi]);
          if constexpr (pp == 1) mf_va<s == 1, AW(s - 1, 2)>(a.V[i], xf[fi]);
          if constexpr (pp == 2) mf_av<s == 1, AW(s - 1, 0)>(a.Q[i], xf[fi]);
        }
        constexpr int wk = (s - 13) * 3;
        if constexpr (s < 16) {
          if constexpr ((n == 0 || n == 1 || n == 4) && !(ABL & 4)) {
            constexpr int k = n == 0 ? 1 : n == 1 ? 2 : 0, o = (s % 4) * 12288 + k * 1024;
            trd_a<AW(s, k), o, o + 512>(wb);
          }
          if constexpr (n == 5 && s > 0) waitl<7>();
          if constexpr (pp == 2 && i < 4) ldx<((s >> 1) & 3) * 10240 + i * 2048>(xf[i], xb[s & 1][s >> 3]);
          if constexpr (n == 13) ldx<((s >> 1) & 3) * 10240 + 8192>(xf[4 + (s & 1)], xb[s & 1][s >> 3]);
        }
        if constexpr (s < 12) { if constexpr (n == 2) x_m0(IC<8 + s>{}); if constexpr (n == 3) x_dma(IC<8 + s>{}); }
        if constexpr (s <= 12) {
          if constexpr (n == 3) m0_add<RING + 24576 + ((s + 3) % 4) * 12288>(w3);
          if constexpr ((n == 6 || n == 9 || n == 12) && !(ABL & 2)) dma<(n / 3 - 2) * 1024>(sW, wvo, so);
          if constexpr (n == 13 && s < 12) so = s_lit<((ABL & 32) ? (s + 4) % 5 : s + 4) * 98304>();
        } else {
          if constexpr (n == 2 || n == 6 || n == 11) so = wo_addr(IC<wk + n / 5>{});
          if constexpr (n == 3 || n == 9 || n == 12) dma<0>(sO, ovo, so);
        }
        if constexpr (n == 7 && (s & 1) && s < 15) *lp<v4u>(onea + (s + 1) / 2 * 10240) = ones;
        if constexpr (n == 14 && s == 3) { asm volatile("" : "+v"(pf)); stc2(x.rS, flo, v2u{e0, e1}); }
        if constexpr (n == 14 && s == 6)
          asm volatile("buffer_load_dwordx4 %0, %2, %4, %5 offen sc1\n\tbuffer_load_dwordx4 %1, %3, %4, %5 offen sc1" : "=&v"(sa), "=&v"(sb) : "v"(sa_o), "v"(sb_o), "s"(x.sS), "s"(W_ST0) : "memory");
        if constexpr (s == 10 && n == 7) { asm volatile("" : "+v"(sa), "+v"(sb)); bad = (sa.z ^ e0) | (sa.w ^ e1); asm volatile("" : "+v"(bad)); }
        if constexpr (s == 10 && n == 8) { bad |= sb.z ^ e0; asm volatile("" : "+v"(bad)); }
        if constexpr (s == 10 && n == 10) { bad |= sb.w ^ e1; asm volatile("" : "+v"(bad)); }
        if constexpr (s == 10 && n == 11) {
          if (RARE(any(bad != 0))) POLL(sa = ldc4(x.rS, sa_o, W_ST0); sb = ldc4(x.rS, sb_o, W_ST0), ((sa.z ^ e0) | (sa.w ^ e1) | (sb.z ^ e0) | (sb.w ^ e1)) != 0);
        }
        if constexpr (s == 11 && n == 8) *lp<unsigned>(M_STM + lane * 4) = sa.x;
        if constexpr (s == 11 && n == 10) { *lp<unsigned>(M_STA + lane * 4) = sa.y; *lp<unsigned>(M_STM + 256 + t * 4) = sb.x; }
        if constexpr (s == 11 && n == 11) *lp<unsigned>(M_STA + 256 + t * 4) = sb.y;
        if constexpr (s == 12 && (n == 2 || n == 7 || n == 8 || n == 10 || n == 11)) pre_stat<(n == 2 ? 0 : n == 7 ? 1 : n == 8 ? 2 : n == 10 ? 3 : 4)>(x, p);
        if constexpr (s > 12 && s < 16 && (n == 7 || n == 10 || n == 13 || n == 14)) pre_stat<5 + (s - 13) * 4 + (n == 7 ? 0 : n == 10 ? 1 : n == 13 ? 2 : 3)>(x, p);
        if constexpr (s == 15 && n == 7) xk[0] = *lp<v4u>(ro);
        if constexpr (s == 15 && n == 10) xk[1] = *lp<v4u>(ro + 2048);
        if constexpr (s == 16 && n < 2) { pre_stat<17 + 2 * n>(x, p); pre_stat<18 + 2 * n>(x, p); }
        SB();
      });
      if constexpr (s == 15) BAR();
    });
  }
  TIE3(3, a.Q[4], a.K[4], a.V[4]);
#pragma unroll
  for (int i = 0; i < 4; ++i) asm volatile("" : "+v"(a.Q[i]), "+v"(a.K[i]), "+v"(a.V[i]));
  STAMP(2);
  x.fresh();
  v4u fl0, fl1;
  const unsigned obs = (8 * x.g + (x.t >> 2)) * 256 + ((4 * w + ((x.lane & 3) ^ (x.g & 1))) ^ (x.t & 12)) * 16 + (x.lane & 1) * 8;
  {
    f32x4 csq, csk;
#pragma unroll
    for (int r = 0; r < 4; ++r) { csq[r] = dppf<0x150>(a.Q[0][r]); csk[r] = dppf<0x150>(a.K[0][r]); }
    float v0 = a.V[0][0], v1 = v0;
    sw32(v0, v1); v1 = v0; sw16(v0, v1);
    *lp<f32x4>(M_CS + w * 192 + x.g * 16) = csq; *lp<f32x4>(M_CS + w * 192 + 64 + x.g * 16) = csk; *lp<float>(M_CS + w * 192 + 128 + x.t * 4) = v0;
    asm volatile("buffer_load_dwordx4 %0, %2, %3, %4 offen sc1\n\tbuffer_load_dwordx4 %1, %2, %3, %4 offen offset:1024 sc1"
                 : "=&v"(fl0), "=&v"(fl1) : "v"(x.lane * 16), "s"(x.sS), "s"(W_FLAGS + w * 2048) : "memory");
    SB();
    Pk k;
    att_prep(a, p, csq, csk, v0, k, [&](auto N) { if constexpr (N.v < 4) issue_wo(IC<12 + N.v>{}); });
    SB();
    att_finish<1, 5>(x, k, p, 0, [&](auto N) { issue_wo(IC<16 + N.v>{}); });
  }
  STAMP(3);
  FS(33);
  if (x.ff >= 0) stc4(x.rS, x.lane * 16, pz, W_Y + (w >> 1) * Y_L + ((w & 1) * 44 + x.ff) * 1024);
  stc4(x.rS, w == 0 ? OOB : small_words(x, 1, pso), pz);
  sfor<24, 28>([&](auto K) { issue_wo(K); });
  waitv<13>();
  BAR();
  FS(34);
  sfor<0, 16>([&](auto J) { constexpr int j = J.v >> 1; trd_a<AO(j, J.v & 1), j * 8192, j * 8192 + 1024>(RING + (obs ^ (8 * (J.v & 1)))); });
  v4u WO[8][2];
#pragma unroll
  for (int ct = 0; ct < 2; ++ct) WO[0][ct] = trd2(O_HI + (obs ^ (8 * ct)), O_HI + (obs ^ (8 * ct)) + 1024);
  SB();
  sfor<28, 32>([&](auto K) { issue_wo(K); });
  asm volatile("" : "+v"(fl0), "+v"(fl1));
#define FLAGS_BAD ((((((fl0.x - e0) | (fl0.z - e0)) | ((fl1.x - e0) | (fl1.z - e0))) >> 1) | ((fl0.y ^ e1) | (fl0.w ^ e1)) | ((fl1.y ^ e1) | (fl1.w ^ e1))) != 0)
  if (RARE(any(FLAGS_BAD))) POLL(fl0 = ldc4(x.rS, x.lane * 16, W_FLAGS + w * 2048); fl1 = ldc4(x.rS, x.lane * 16 + 1024, W_FLAGS + w * 2048), FLAGS_BAD);
  f32x4 res[2][2];
#pragma unroll
  for (int T = 0; T < 2; ++T) unpack8(halves(x, xk[T]), res[T][0], res[T][1]);
  TIE0_4(res[0][0], res[0][1], res[1][0], res[1][1]);
  unsigned pf3 = 0;
  u64 pfa;
  {
    const int n = ((jb >> 2) * 3 + x.q - 1) * 4 + w;
    const unsigned og = min(n * 6 + (x.lane >> 2) % 6, 511) * 2752 + min(xc * 384 + min((x.lane & 3) * 128, 380), 2748), od = min(xc * 196608 + (n * 16 + x.lane - 48) * 128, 1376 * 1024 - 128);
    pfa = x.lane < 48 ? (x.lane < 24 ? (u64)wg : (u64)wu) + og : (u64)wd + od;
    if (x.q == 0 || !PF2) pfa = (u64)wo;
  }
  if (PFM == 5) pf3 = touch(pfa, 0xffffffffffffull);
  OReq rq = o_plan(x, 0);
  SB();
  FS(35);
  waitv<(PFM == 5 ? 1 : 0)>();
  BAR();
  FS(36);
  STAMP(4);
  if (DLY0) __builtin_amdgcn_s_sleep(DLY0);
  o_issue(x, rq);
  if (PFM == 6) pf3 = touch(pfa, 0xffffffffffffull);
#pragma unroll
  for (int j = 9; j < 16; ++j)
#pragma unroll
    for (int ct = 0; ct < 2; ++ct) WO[j - 8][ct] = trd2(WO_X + (obs ^ (8 * ct)) + (j - 9) * 8192, WO_X + (obs ^ (8 * ct)) + (j - 9) * 8192 + 1024);
  SB();
  asm volatile("" : "+v"(pfa));
  const v4u sG = mk_srd(wg, 512 * 2752), sU = mk_srd(wu, 512 * 2752);
  const unsigned gvo = (x.lane >> 2) * 2752 + x.ff * 64 + (((((x.lane >> 1) & 1) ^ (x.lane >> 5)) * 2) + (x.lane & 1)) * 16;
  outproj<0, (PFM == 6 ? 1 : 0)>(x, rq, WO, res, wfa, wfb, wsum, xk, pfa, [&]() {
    if (PFM >= 2 && PFM < 5) pf3 = touch(pfa, PFM == 2 ? ~0ull : ~PF_EARLY);
    if (x.ff >= 0) {
      sfor<0, 8>([&](auto K) { dma_at<K.v * 1024, K.v * 16 * 2752>(sG, gvo, RING + w * 8192, w * 128 * 2752); });
      waitv<(PFM >= 2 && PFM < 5 ? 9 : 8)>();
    } else waitv<(PFM >= 2 && PFM < 5 ? 1 : 0)>();
  });
  STAMP(9);
  FS(42);
  x.fresh();
  const v4u sD = mk_srd(wd, 1376 * 1024);
  unsigned k1 = 0, k2 = 0;
  auto issue_x1 = [&](int t0, int t1) {
#pragma unroll
    for (int k = t0; k < t1; ++k) dma4c(x.sS, x.lane * 16, XIMG + k * 16384 + w4, W_X + max(4 * c - 1 + k, 0) * 16384 + w4);
  };
  v2u ky, sa, sb;
  const unsigned sa_o = max(64 * c - 16 + x.lane, 0) * 8, sb_o = (64 * c + 48 + x.t) * 8;
  v4u xf[4];
  auto tile = [&](auto I, unsigned b0, unsigned nb, auto&& fill) {
    constexpr int i = I.v;
    sfor<0, 16>([&](auto S) {
      constexpr int s = S.v;
      mf_av<s == 0, AW(s, 1)>(a.K[i], xf[s & 3]);
      xf[(s + 3) & 3] = *lp<v4u>(s < 13 ? b0 + (s + 3) * 1024 : nb + (s - 13) * 1024);
      SB();
      mf_va<s == 0, AW(s, 2)>(a.V[i], xf[s & 3]);
      fill(IC<2 * s>{});
      SB();
      if constexpr (i > 0) mf_av<s == 0, AW(s, 0)>(a.Q[i], xf[s & 3]);
      fill(IC<2 * s + 1>{});
      SB();
    });
  };
  auto first3 = [&](unsigned b0) {
#pragma unroll
    for (int s = 0; s < 3; ++s) xf[s] = *lp<v4u>(b0 + s * 1024);
  };
  auto none = [](auto) {};
  auto mark_o = [&](int u) {
    stc4(x.rS, x.lane * 16, pz, W_O + BIG_L + (c * 64 + 32 * u + 4 * hg + w) * 1024); stc4(x.rS, x.lane * 16, pz, W_X + BIG_L + ((2 * gi + u) * 16 + 4 * x.q + w) * 1024);
  };
  const unsigned xb = XIMG + x.lane * 16;
  auto rows_ready = [&]() {
    if (w == 1) {
      const unsigned o = x.t < 12 ? (max(2 * c - 1 + (x.t >> 2), 0) * 4 + (x.t & 3)) * 128 : OOB;
      unsigned v;
      if (DXF) __builtin_amdgcn_s_sleep(DXF);
      POLL(v = ldc1(x.rS, o, W_XF), v == POISON);
    }
    __builtin_amdgcn_s_barrier();
    MEMF();
    STAMP(10);
    FS(43);
  };
  int Ra, Rb, ta, tb, last;
  int rb = 2047 + 16 - 64 * c, rlo = c == 0 ? 16 : 2, rn = 80 - rlo;
  asm volatile("" : "+s"(rb), "+s"(rlo), "+s"(rn));
  auto place = [&](unsigned k, int& R, int& t) { R = rb - (int)(k & 0x7FFu); t = (unsigned)(R - rlo) < (unsigned)rn ? R >> 4 : -1; pin_s(R); pin_s(t); };
  unsigned kh, kl, oh, ol;
  auto winners = [&](auto N) {
    constexpr int n = N.v, st = n >> 1, C = st < 4 ? 0x110 + (1 << st) : 0x142 + (st - 4), RM = st < 4 ? 0xF : (st == 4 ? 0xA : 0xC);
    if constexpr (n == 12) { k1 = (unsigned)__builtin_amdgcn_readlane((int)kh, 63); k2 = (unsigned)__builtin_amdgcn_readlane((int)kl, 63); }
    else if constexpr (n & 1) { kl = max(min(kh, oh), ol); kh = max(kh, oh); pin_v(kl); pin_v(kh); }
    else { oh = (unsigned)__builtin_amdgcn_update_dpp(0, (int)kh, C, RM, 0xF, false); ol = dmax<C, RM>(kl); pin_v(oh); pin_v(ol); }
  };
  if (x.ff >= 0) {
    v4u raw[8]; Ffn F;
    unsigned xfl; int tka, tkb; unsigned so3, so4;
    sfor<0, 8>([&](auto K) { dma_at<32768 + K.v * 1024, K.v * 16 * 2752>(sU, gvo, RING + w * 8192, w * 128 * 2752); });
    sfor<0, 10>([&](auto N) { ffn_const<N.v>(x, F); });
    rows_ready();
    issue_x1(0, 2);
    ld_xf(x, xfl); ld_key(x, ky);
    waitv<6>();
    __builtin_amdgcn_s_barrier();
    MEMF();
    STAMP(28);
    FS(44);
    first3(xb);
    tile(IC<0>{}, xb, xb + 16384, [&](auto N) {
      constexpr int n = N.v;
      if constexpr (n == 0 || n == 8) ffn_load_down_half<n / 8>(x, sD, raw);
      if constexpr (n == 14) {
        waitv<8>(); pin_v(xfl); tie1(ky);
        if (any(max(xfl, ky.x) == POISON)) POLL(xfl = ldc1(x.rS, x.lane * 128, W_XF + w * 8192); ky = ldc2(x.rS, x.lane * 128, W_KEY), max(xfl, ky.x) == POISON);
        kh = ky.x; kl = ky.y;
      }
      if constexpr (n == 16) { __builtin_amdgcn_s_barrier(); MEMF(); }
      if constexpr (n >= 18 && !(n & 1)) winners(IC<(n - 18) / 2>{});
    });
    FS(45);
    tile(IC<1>{}, xb + 16384, xb, [&](auto N) {
      constexpr int n = N.v;
      if constexpr (n < 6) winners(IC<7 + n>{});
      if constexpr (n == 7) { tka = key_tok(k1); tkb = key_tok(k2); ffn_row_addr(x, F, tka, tkb); }
      if constexpr (n >= 8 && n < 12) ffn_row<n - 8>(x, F, 0);
      if constexpr (n == 12) ffn_row_stats(x, F, 0, tka, tkb);
      if constexpr (n == 13) place(k1, Ra, ta);
      if constexpr (n == 14) place(k2, Rb, tb);
      if constexpr (n == 15) { last = max(max(ta, tb), 1); last = last > 1 ? last : 4; pin_s(last); }
      if constexpr (n == 16) { const int t3 = last == 2 ? 4 : 2; so3 = W_X + (4 * c - 1 + t3) * 16384 + w4; so4 = W_X + (4 * c + 5 - t3) * 16384 + w4; set_m0(XIMG + t3 * 16384 + w4); }
      if constexpr (n >= 17 && n < 21) dmac<(n - 17) * 1024>(x.sS, x.lane * 16, so3);
      if constexpr (n == 21) ld_stat80(x, sa_o, sb_o, sa, sb);
      if constexpr (n == 22) { F.sg = key_sig((x.g & 1) ? k2 : k1); pin_f(F.sg); }
      if constexpr (n == 23) { waitv<11>(); ffn_down_landed(raw); set_m0(XIMG + 3 * 16384 + w4); }
      if constexpr (n >= 24) ffn_perm<n - 24>(raw, F);
    });
    STAMP(11);
    FS(50);
    ffn_gate_in(F);
    SB();
    waitv<6>();
    FS(51);
    TIE0_4(F.xf[0], F.xf[1], F.xf[2], F.xf[3]); tie1(F.st);
    ffn_core(x, F, 0, [&](auto N) {
      constexpr int n = N.v;
      if constexpr (n < 4) dmac<n * 1024>(x.sS, x.lane * 16, W_X + (4 * c + 2) * 16384 + w4);
      if constexpr (n == 4) { set_m0(XIMG + (last == 2 ? 2 : 4) * 16384 + w4); waitv<4>(); tie2(sa, sb); }
      if constexpr (n > 4 && n < 9) dmac<(n - 5) * 1024>(x.sS, x.lane * 16, so4);
    });
    STAMP(12);
    FS(54);
    first3(xb + (last == 2 ? 4 : 2) * 16384);
    mark_o(0); mark_o(1);
  } else {
    if (b == 0 && w == 3) stc2(x.rS, x.lane == 0 ? W_EPOCH : OOB, v2u{e0 + 1u, e1});
    rows_ready();
    issue_x1(0, 2);
    ld_key(x, ky);
    issue_x1(2, 3);
    ld_stat80(x, sa_o, sb_o, sa, sb);
    issue_x1(3, 5);
    mark_o(0); mark_o(1);
    waitv<18>();
    tie1(ky);
    __builtin_amdgcn_s_barrier();
    MEMF();
    STAMP(28);
    FS(44);
    first3(xb);
    tile(IC<0>{}, xb, xb + 16384, none);
    tile(IC<1>{}, xb + 16384, xb + 32768, [&](auto N) {
      constexpr int n = N.v;
      if constexpr (n == 0) { if (RARE(any(ky.x == POISON))) POLL(ky = ldc2(x.rS, x.lane * 128, W_KEY), ky.x == POISON); kh = ky.x; kl = ky.y; }
      if constexpr (n >= 1 && n < 14) winners(IC<n - 1>{});
      if constexpr (n == 15) place(k1, Ra, ta);
      if constexpr (n == 17) place(k2, Rb, tb);
      if constexpr (n == 19) { last = max(max(ta, tb), 1); last = last > 1 ? last : 4; pin_s(last); }
      if constexpr (n == 24) { waitv<12>(); __builtin_amdgcn_s_barrier(); MEMF(); tie2(sa, sb); }
    });
    if (last == 2) { DRAIN(); __builtin_amdgcn_s_barrier(); MEMF(); first3(xb + 65536); }
  }
  const int hi = max(ta, tb), lo = min(ta, tb);
  const bool ina = ta >= 0;
  f32x4 csq, csk; float csv;
  {
    const unsigned b2 = xb + (last == 2 ? 4 : 2) * 16384, b3 = xb + (last == 3 ? 4 : 3) * 16384, b4 = xb + last * 16384;
    if (RARE(any(max(sa.x, sb.x) == POISON))) POLL(sa = ldc2(x.rS, sa_o, W_ST); sb = ldc2(x.rS, sb_o, W_ST), max(sa.x, sb.x) == POISON);
    FS(55);
    float ra, rb; unsigned flo;
    tile(IC<2>{}, b2, b3, [&](auto S) {
      constexpr int s = (S.v & 1) ? -1 : S.v / 2;
      if constexpr (s == 0) { ra = rsq(f_(sa.y) + EPS); asm volatile("" : "+v"(ra)); }
      if constexpr (s == 1) { rb = rsq(f_(sb.y) + EPS); asm volatile("" : "+v"(rb)); }
      if constexpr (s == 2) { *lp<float>(M_RS + x.lane * 4) = ra; ra += 1.f; asm volatile("" : "+v"(ra)); }
      if constexpr (s == 3) { *lp<float>(M_RS + 256 + x.t * 4) = rb; rb += 1.f; asm volatile("" : "+v"(rb)); }
      if constexpr (s == 4) *lp<unsigned>(M_STM + x.lane * 4) = sa.x;
      if constexpr (s == 5) *lp<unsigned>(M_STM + 256 + x.t * 4) = sb.x;
      if constexpr (s == 6) { sa.x = u_(f_(sa.y) * ra); asm volatile("" : "+v"(sa)); }
      if constexpr (s == 7) { sb.x = u_(f_(sb.y) * rb); asm volatile("" : "+v"(sb)); }
      if constexpr (s == 8) { sa.x = u_(rsq(f_(sa.x) * ra + EPS)); asm volatile("" : "+v"(sa)); }
      if constexpr (s == 9) { sb.x = u_(rsq(f_(sb.x) * rb + EPS)); asm volatile("" : "+v"(sb)); }
      if constexpr (s == 10) *lp<float>(M_STA + x.lane * 4) = ra * f_(sa.x);
      if constexpr (s == 11) *lp<float>(M_STA + 256 + x.t * 4) = rb * f_(sb.x);
      if constexpr (S.v == 24) { DRAIN(); __builtin_amdgcn_s_barrier(); MEMF(); }
      if constexpr (S.v == 25) pin_v(pf3);
      if constexpr (S.v == 25) { flo = x.lane == 0 ? W_FLAGS + (b * 4 + w) * 8 : OOB; pin_v(flo); }
      if constexpr (S.v == 27) stc2(x.rS, flo, v2u{e0 + 1u, e1});
    });
    x.fresh();
    auto consts_in = [&](auto N) {
      constexpr int n = N.v;
      if constexpr (n == 0) p.bc = *lp<f32x4>(M_BC + x.lane * 16);
      if constexpr (n == 1) p.bp = *lp<f32x4>(M_BP + x.lane * 16);
      if constexpr (n == 2) csv = *lp<float>(M_CS + w * 192 + 128 + x.t * 4);
      if constexpr (n == 3) csq = *lp<f32x4>(M_CS + w * 192 + x.g * 16);
      if constexpr (n == 4) csk = *lp<f32x4>(M_CS + w * 192 + 64 + x.g * 16);
    };
    if (hi < 0) {
      tile(IC<3>{}, b3, b4, none);
      tile(IC<4>{}, b4, xb, [&](auto N) { if constexpr (N.v < 21) pre_stat<N.v>(x, p); else consts_in(IC<N.v - 21>{}); });
    } else {
      const int t = x.t, g = x.g;
      const unsigned fs = g < 2 ? M_FST + w * 8 : M_DUMP;
      auto patch = [&](int sl, auto EARLY) {
        const int R = sl ? Rb : Ra;
        unsigned o = (sl * 44 + g) * 1024 + w * 256 + t * 16, o10, xa, fw;
        v4u y[11], sel;
        v2u xr; float m1, rs1, fa, fb; f32x4 x2;
        auto step = [&](auto N) {
          constexpr int n = N.v;
          if constexpr (!(n & 1) && n < 20) y[n / 2] = ldc4(x.rS, o, W_Y + n / 2 * 4096);
          if constexpr (n == 20) y[10] = ldc4(x.rS, o10, W_Y + 40960);
          if constexpr (n == 1) { xa = (4 * w + (t >> 2)) * 1024 + (t & 3) * 256; pin_v(xa); }
          if constexpr (n == 3) { xa += XIMG + (R >> 4) * 16384 + (R & 15) * 16 + (g & 1) * 8; pin_v(xa); }
          if constexpr (n == 5) { xr = *lp<v2u>(xa); m1 = *lp<float>(M_STM + R * 4); }
          if constexpr (n == 7) { rs1 = *lp<float>(M_RS + R * 4); o10 = g == 3 ? OOB : o; pin_v(o10); }
          if constexpr (n == 9) { fw = g < 2 ? xa : M_DUMP; pin_v(fw); }
          if constexpr (n == 11) { unsigned v = (t & 1) ? 0x3F800000u : 0x00003F80u; pin_v(v); sel.w = v; }
          if constexpr (n == 13) { unsigned v = (t >> 1) == 0 ? sel.w : 0u; pin_v(v); sel.x = v; }
          if constexpr (n == 15) { unsigned v = (t >> 1) == 1 ? sel.w : 0u; pin_v(v); sel.y = v; }
          if constexpr (n == 17) { unsigned v = (t >> 1) == 2 ? sel.w : 0u; pin_v(v); sel.z = v; }
          if constexpr (n == 19) { unsigned v = (t >> 1) == 3 ? sel.w : 0u; pin_v(v); sel.w = v; }
          if constexpr (n == 21) { fa = 1.f + rs1; fb = -m1 * rs1; pin_f2(fa, fb); }
          if constexpr (n >= 22 && n < 26) { float v = ((n & 1) ? bhi(xr[(n - 22) >> 1]) : blo(xr[(n - 22) >> 1])) * fa + fb; pin_f(v); x2[n - 22] = v; }
          if constexpr (EARLY.v && n >= 26) consts_in(IC<n - 26>{});
        };
        if constexpr (EARLY.v) { tile(IC<3>{}, b3, b4, step); STAMP(14); } else sfor<0, 26>(step);
#define Y_BAD (max(max(max(max(y[0].x, y[1].x), max(y[2].x, y[3].x)), max(max(y[4].x, y[5].x), max(y[6].x, y[7].x))), max(max(y[8].x, y[9].x), y[10].x)) == POISON)
        if (any(Y_BAD)) {
          if (YS) { unsigned v; const unsigned so = x.lane < NFF ? (sl * 44 + x.lane) * 1024 + w * 256 + 240 : OOB; POLL(v = ldc1(x.rS, so, W_Y), v == POISON); }
          POLL(if (NAPY) __builtin_amdgcn_s_sleep(NAPY); _Pragma("unroll") for (int i = 0; i < 11; ++i) y[i] = ldc4(x.rS, i == 10 ? o10 : o, W_Y + i * 4096), Y_BAD);
        }
        STAMP(15);
#pragma unroll
        for (int i = 0; i < 11; ++i) { if (i == 0) mf_vvn<false>(x2, sel, y[i]); else mf_vv<false>(x2, sel, y[i]); }
        tie_nop(x2);
        *lp<v2u>(fw) = v2u{pk(x2[0], x2[1]), pk(x2[2], x2[3])};
        float s1 = rowsum((x2[0] + x2[1]) + (x2[2] + x2[3])), s2 = rowsum((x2[0] * x2[0] + x2[1] * x2[1]) + (x2[2] * x2[2] + x2[3] * x2[3]));
        sw16(s1, s2);
        *lp<float>(fs + sl * 32 + g * 4) = s1 + s2;
      };
      auto row_stats = [&](int sl) {
        const int R = sl ? Rb : Ra;
        const f32x4 fa = *lp<f32x4>(M_FST + sl * 32), fb = *lp<f32x4>(M_FST + sl * 32 + 16);
        const float m2 = ((fa[0] + fa[2]) + (fb[0] + fb[2])) * (1.f / 512);
        *lp<float>(M_STM + R * 4) = m2; *lp<float>(M_STA + R * 4) = rsq(fmaxf(((fa[1] + fa[3]) + (fb[1] + fb[3])) * (1.f / 512) - m2 * m2, 0.f) + EPS);
      };
      const int s0 = ina ? 0 : 1, R0 = ina ? Ra : Rb;
      patch(s0, IC<1>{});
      if (RARE(lo >= 0)) patch(1, IC<0>{});
      BAR();
      first3(b4);
      if (RARE(lo >= 0)) row_stats(1);
      STAMP(16);
      f32x4 fa, fb; float m2, v2;
      tile(IC<4>{}, b4, xb, [&](auto N) {
        constexpr int n = N.v;
        if constexpr (n == 0) fa = *lp<f32x4>(M_FST + s0 * 32);
        if constexpr (n == 1) fb = *lp<f32x4>(M_FST + s0 * 32 + 16);
        if constexpr (n == 3) { m2 = fa[0] + fa[2]; v2 = fa[1] + fa[3]; }
        if constexpr (n == 4) { m2 += fb[0]; v2 += fb[1]; }
        if constexpr (n == 5) { m2 += fb[2]; v2 += fb[3]; }
        if constexpr (n == 6) { m2 *= 1.f / 512; v2 = v2 * (1.f / 512) + EPS; }
        if constexpr (n == 7) { *lp<float>(M_STM + R0 * 4) = m2; v2 -= m2 * m2; }
        if constexpr (n == 8) v2 = rsq(fmaxf(v2, EPS));
        if constexpr (n >= 3 && n < 9) pin_f2(m2, v2);
        if constexpr (n == 9) *lp<float>(M_STA + R0 * 4) = v2;
        if constexpr (n >= 10 && n < 31) pre_stat<n - 10>(x, p);
      });
    }
    if (ta == 0 || tb == 0) { tile(IC<0>{}, xb, xb, none); TIE2(7, a.K[0], a.V[0]); }
    if (ta == 1 || tb == 1) { first3(xb + 16384); tile(IC<1>{}, xb + 16384, xb, none); TIE3(7, a.Q[1], a.K[1], a.V[1]); }
    if (RARE(lo > 1 && lo != hi)) {
      first3(xb + lo * 16384);
      if (lo == 2) { tile(IC<2>{}, xb + 32768, xb, none); TIE3(7, a.Q[2], a.K[2], a.V[2]); } else { tile(IC<3>{}, xb + 49152, xb, none); TIE3(7, a.Q[3], a.K[3], a.V[3]); }
    }
  }
  TIE3(7, a.Q[4], a.K[4], a.V[4]);
#pragma unroll
  for (int i = 1; i < 4; ++i) asm volatile("" : "+v"(a.Q[i]), "+v"(a.K[i]), "+v"(a.V[i]));
  TIE0_2(a.K[0], a.V[0]);
#pragma unroll
  for (int i = 2; i < 4; ++i)
    if (last == i) { swap4(a.Q[i], a.Q[4]); swap4(a.K[i], a.K[4]); swap4(a.V[i], a.V[4]); }
  STAMP(13);
  x.fresh();
  v2u f2;
  const unsigned fo = (((b & ~0x38) + (x.lane >> 2 & 7) * 8) * 4 + (x.lane & 3)) * 8;
  {
#pragma unroll
    for (int r = 0; r < 4; ++r) p.b0[r] = c == 0 ? -1e30f : p.bp[r];
    p.slo = (x.t & 1) ? 0x01000c0cu : 0x0c0c0100u; p.shi = p.slo + ((x.t & 1) ? 0x02020000u : 0x00000202u);
    ld_flags(x, fo, f2);
    SB();
    Pk k;
    att_prep(a, p, csq, csk, csv, k, none);
    SB();
    att_finish<1, 5>(x, k, p, 1, none);
  }
  STAMP(17);
  v2u s0 = {POISON, POISON}, s1 = s0;
  auto tail = [&](auto FFN) {
    x.fresh();
#pragma unroll
    for (int T = 0; T < 2; ++T) {
      const int R = 16 + 32 * x.u + 16 * T + x.t;
      const float m = *lp<float>(M_STM + R * 4), rs = *lp<float>(M_RS + R * 4);
      const bool routed = R == Ra || R == Rb;
      const v4u xi = *lp<v4u>(XIMG + ((1 + 2 * x.u + T) * 16 + 4 * x.q + w) * 1024 + x.lane * 16);
      const float fa = routed ? 1.f : 1.f + rs, fb = routed ? 0.f : -m * rs;
      unpack8(halves(x, xi), res[T][0], res[T][1]);
      res[T][0] = res[T][0] * fa + fb; res[T][1] = res[T][1] * fa + fb;
    }
    TIE0_4(res[0][0], res[0][1], res[1][0], res[1][1]);
    rq = o_plan(x, 1);
    waitv<2>();
    tie1(f2);
    if (any(((f2.x ^ (e0 + 1u)) | (f2.y ^ e1)) != 0)) POLL(__builtin_amdgcn_s_sleep(NAP); f2 = ldc2(x.rS, fo, W_FLAGS), ((f2.x ^ (e0 + 1u)) | (f2.y ^ e1)) != 0);
    DRAIN();
    BAR();
    STAMP(18);
    if (DLY1) __builtin_amdgcn_s_sleep(DLY1);
    o_issue(x, rq);
    v4u raw[8]; Ffn F;
    if constexpr (FFN.v) ffn_load_down(x, sD, raw);
    outproj<1, (FFN.v ? 8 : 0)>(x, rq, WO, res, wfa, wfb, wsum, xk, 0, [&]() {});
    STAMP(21);
    if constexpr (FFN.v) {
      x.fresh();
      waitv<2>();
      ffn_down_landed(raw);
      sfor<0, 8>([&](auto C) { ffn_perm<C.v>(raw, F); });
      sfor<0, 10>([&](auto N) { ffn_const<N.v>(x, F); });
      ffn_gate_in(F);
      Late lt;
      {
        const int a = key_tok(k1), b = key_tok(k2);
        lt.lc[0] = a >> 6; lt.lc[1] = ((a & 63) >= 50 && a < 1984) ? (a >> 6) + 1 : -1;
        lt.lc[2] = ((b >> 6) == lt.lc[0] || (b >> 6) == lt.lc[1]) ? -1 : b >> 6;
        lt.lc[3] = ((b & 63) >= 50 && b < 1984 && (b >> 6) + 1 != lt.lc[0] && (b >> 6) + 1 != lt.lc[1]) ? (b >> 6) + 1 : -1;
        pin_s(lt.lc[0]); pin_s(lt.lc[1]); pin_s(lt.lc[2]); pin_s(lt.lc[3]);
      }
      top2_late(x, lt, wsum, k1, k2);
      STAMP(23);
      const int tka = key_tok(k1), tkb = key_tok(k2);
      ffn_row_addr(x, F, tka, tkb);
      sfor<0, 4>([&](auto S) { ffn_row<S.v>(x, F, 1); });
      ffn_row_stats(x, F, 1, tka, tkb);
      F.sg = key_sig((x.g & 1) ? k2 : k1);
      int wa = -1, wb = -1;
#pragma unroll
      for (int i = 0; i < 4; ++i) { wa = (tka >> 6) == lt.lc[i] ? i : wa; wb = (tkb >> 6) == lt.lc[i] ? i : wb; }
      const int wl = (x.g & 1) ? wb : wa;
      v2u la = *lp<v2u>(M_LST + max(wl, 0) * 512 + (F.so & 511));
      pin_f(F.sg); tie1(la);
      DRAIN();
      tie4(F.xf[0], F.xf[1], F.xf[2], F.xf[3]); tie1(F.st);
      F.st = wl >= 0 ? la : F.st;
      ffn_core(x, F, 1, [&](auto N) {
        if constexpr (N.v == 0) stc2(x.rS, (x.ff < 8 && w == 3 && x.lane == 0) ? x.ff * 128 : OOB, v2u{k1, k2}, W_RES);
        if constexpr (N.v == 2) ld_stats(x, W_ST + ST_L + gi * 256, s0, s1);
      });
      waitv<1>();
      tie2(s0, s1);
      STAMP(24);
    }
  };
  if (x.ff >= 0) tail(IC<1>{}); else tail(IC<0>{});
  x.fresh();
  const rsrc_t rOut = mk_rs(out, 2048 * 1024);
  const unsigned oso = 32 * gi * 1024 + n0 * 2;
  {
    if (x.ff < 0) DRAIN();
    if (any(max(s0.x, s1.x) == POISON)) POLL(__builtin_amdgcn_s_sleep(NAP); s0 = ldc2(x.rS, x.t * 8, W_ST + ST_L + gi * 256); s1 = ldc2(x.rS, x.t * 8 + 128, W_ST + ST_L + gi * 256), max(s0.x, s1.x) == POISON);
#pragma unroll
    for (int T = 0; T < 2; ++T) {
      const float m = f_(T ? s1.x : s0.x), rs = rsq(f_(T ? s1.y : s0.y) + EPS);
      unpack8(xk[T], res[T][0], res[T][1]);
      res[T][0] = res[T][0] * (1.f + rs) - m * rs; res[T][1] = res[T][1] * (1.f + rs) - m * rs;
      __builtin_amdgcn_raw_buffer_store_b128(pack8(res[T][0], res[T][1]), rOut, (16 * T + x.t) * 1024 + x.g * 16, oso, 0);
    }
  }
  STAMP(22);
  if (x.ff < 0) {
    v2u k;
    POLL(__builtin_amdgcn_s_sleep(2); k = ldc2(x.rS, 0, W_RES + ((xc + 1) & 7) * 128), k.x == POISON);
    k1 = rfl(k.x); k2 = rfl(k.y);
  }
  STAMP(25);
#pragma unroll 1
  for (int sl = 0; sl < 2; ++sl) {
    const int R = key_tok(sl ? k2 : k1) - 32 * gi;
    if (R < 0 || R >= 32) continue;
    y_slice(x, 1, sl);
    const f32x4 ya = *lp<f32x4>(M_YS + w * 128 + x.g * 32), yb = *lp<f32x4>(M_YS + w * 128 + x.g * 32 + 16);
    const f32x4 a0 = (R < 16 ? res[0][0] : res[1][0]) + ya, a1 = (R < 16 ? res[0][1] : res[1][1]) + yb;
    __builtin_amdgcn_raw_buffer_store_b128(pack8(a0, a1), rOut, x.t == (R & 15) ? R * 1024 + x.g * 16 : OOB, oso, 0);
  }
  asm volatile(".Lfused_end:\n\ts_nop 0\n\ts_nop 0");
  STAMP(26);
}
}

extern "C" void kernel_launch(void* const* d_in, const int* in_sizes, int n_in, void* d_out, int out_size, void* d_ws, size_t ws_size, hipStream_t stream) {
  fused<<<256, 256, 0, stream>>>((const u16*)d_in[0], (const u16*)d_in[1], (const u16*)d_in[2], (const u16*)d_in[4], (const u16*)d_in[5],
                                 (const u16*)d_in[6], (const u16*)d_in[7], (u16*)d_out, (char*)d_ws);
}
```
